```python
import torch

def _softmax(x, axis=-1):
    return torch.softmax(x, dim=axis)
def _gelu(x):
    return torch.nn.functional.gelu(x, approximate="tanh")
def _silu(x):
    return torch.nn.functional.silu(x)
def _sigmoid(x):
    return torch.sigmoid(x)
def _layernorm(x, axis=-1, eps=1e-5):
    m = x.mean(dim=axis, keepdim=True)
    v = x.var(dim=axis, keepdim=True, unbiased=False)
    return (x - m) / torch.sqrt(v + eps)
def _scatter_add(tgt, idx, src, axis=0):
    return tgt.clone().index_add_(axis, idx, src)
def _segment_sum(src, seg, n):
    out = torch.zeros((n,) + src.shape[1:], dtype=src.dtype)
    return out.index_add_(0, seg, src)
def _pad(x, pw):
    flat = tuple(v for pair in reversed(pw) for v in pair)
    return torch.nn.functional.pad(x, flat)

kernel_name = 'gated_mix__mha_block_cached__dense_activation_silu__decode'

def setup_inputs():

    _w = (lambda a: (lambda t: t.to(torch.bfloat16) if t.is_floating_point() else t)(torch.as_tensor(a)))
    def _A_setup():
        B, Sq, D = 2, 1, 640
        H, Sk, Dh = 32, 4096, 128
        return {
            "x": _w((lambda *s: torch.randn(*s, dtype=torch.bfloat16) * 0.05)(B, Sq, D)),
            "k_cache": _w((lambda *s: torch.randn(*s, dtype=torch.bfloat16) * 6.4)(B, H, Sk, Dh)),
            "v_cache": _w((lambda *s: torch.randn(*s, dtype=torch.bfloat16) * 0.2)(B, H, Sk, Dh)),
            "wq": _w((lambda *s: torch.randn(*s, dtype=torch.bfloat16) * 6.4)(D, H * Dh) * (D ** -0.5)),
            "wo": _w((lambda *s: torch.randn(*s, dtype=torch.bfloat16) * 0.2)(H * Dh, D) * ((H * Dh) ** -0.5)),
        }
    _A_in = _A_setup()

    def _B_setup():
        B, S, D, F = 2, 1, 640, 1728
        return {
            "x": _w((lambda *s: torch.randn(*s, dtype=torch.bfloat16) * 0.05)(B, S, D)),
            "w1": _w((lambda *s: torch.randn(*s, dtype=torch.bfloat16) * 6.4)(D, F) * (D ** -0.5)),
            "w2": _w((lambda *s: torch.randn(*s, dtype=torch.bfloat16) * 0.05)(F, D) * (F ** -0.5)),
        }
    _B_in = _B_setup()

    return {
        "A_x": _A_in["x"],
        "A_k_cache": _A_in["k_cache"],
        "A_v_cache": _A_in["v_cache"],
        "A_wq": _A_in["wq"],
        "A_wo": _A_in["wo"],
        "B_x": _B_in["x"],
        "B_w1": _B_in["w1"],
        "B_w2": _B_in["w2"],
        "gate_w": _w((lambda *s: torch.randn(*s, dtype=torch.bfloat16) * 0.05)(640) * 16.0),
    }

def reference(A_x, A_k_cache, A_v_cache, A_wq, A_wo, B_x, B_w1, B_w2, gate_w):
    def _A_ref(x, k_cache, v_cache, wq, wo):
        B, Sq, D = x.shape
        H, Dh = 32, 128
        q = (lambda x, *d: x.permute(*d))((x @ wq).reshape(B, Sq, H, Dh), 0, 2, 1, 3)
        scores = torch.einsum("bhqd,bhkd->bhqk", q, k_cache) * (Dh ** -0.5)
        p = _softmax(scores, axis=-1)
        o = torch.einsum("bhqk,bhkd->bhqd", p, v_cache)
        return (lambda x, *d: x.permute(*d))(o, 0, 2, 1, 3).reshape(B, Sq, H * Dh) @ wo

    def _B_ref(x, w1, w2):
        h = torch.einsum("bsd,df->bsf", x, w1)
        h = _silu(h)
        out = torch.einsum("bsf,fd->bsd", h, w2)
        return out

    x = A_x
    g = _sigmoid((x * gate_w).sum(-1, keepdims=True))
    a = _A_ref(x, A_k_cache, A_v_cache, A_wq, A_wo)
    b = _B_ref(x, B_w1, B_w2)
    return g * a + (1 - g) * b
```

```cpp
#include <hip/hip_runtime.h>
#include <hip/hip_bf16.h>
#include <stdint.h>

typedef unsigned int u32;
typedef unsigned short u16;
typedef unsigned long long u64;
using u32x4 = __attribute__((ext_vector_type(4))) unsigned;
using f32x4 = __attribute__((ext_vector_type(4))) float;
using bf16x2 = __attribute__((ext_vector_type(2))) __bf16;
#define GAS __attribute__((address_space(1)))
typedef GAS u64 gu64;
#define RLX_AGENT __ATOMIC_RELAXED, __HIP_MEMORY_SCOPE_AGENT

constexpr int DM = 640, NH = 32, SK = 4096, DH = 128, FF = 1728;
constexpr int NS = 8;
constexpr int FH = FF / NH;
constexpr int CS = DM / NS;
constexpr size_t WS_E = 0;
constexpr size_t WS_FLAG = 4096;
constexpr size_t WS_BOX = 65536;
constexpr int A_GRAN = 2 * 130 + 2 * FH;
constexpr int B_OFF = 384;
constexpr int BOX_GRAN = B_OFF + 2 * CS;
constexpr size_t BOX_BYTES = 4608;
constexpr u32 SPIN_CAP = 1u << 18;
constexpr int KEARLY = 11;

__device__ __forceinline__ float dot2(u32 k, u32 q, float acc) {
  return __builtin_amdgcn_fdot2_f32_bf16(__builtin_bit_cast(bf16x2, k), __builtin_bit_cast(bf16x2, q), acc, false);
}
__device__ __forceinline__ float bflo(u32 v) { return __builtin_bit_cast(float, v << 16); }
__device__ __forceinline__ float bfhi(u32 v) { return __builtin_bit_cast(float, v & 0xffff0000u); }
__device__ __forceinline__ float bf16r(float x) { return (float)(__bf16)x; }
__device__ __forceinline__ u16 bf16bits(float x) { return __builtin_bit_cast(u16, (__bf16)x); }
template <int CTRL> __device__ __forceinline__ float dpp(float x) {
  return __builtin_bit_cast(float, __builtin_amdgcn_mov_dpp(__builtin_bit_cast(int, x), CTRL, 0xf, 0xf, true));
}
constexpr int XOR1 = 0xB1, XOR2 = 0x4E, XOR7 = 0x141, XOR8 = 0x128;
__device__ __forceinline__ float x16_sum(float x) {
  auto s = __builtin_amdgcn_permlane16_swap(__float_as_uint(x), __float_as_uint(x), false, false);
  return __uint_as_float(s[0]) + __uint_as_float(s[1]);
}
__device__ __forceinline__ float x32_sum(float x) {
  auto t = __builtin_amdgcn_permlane32_swap(__float_as_uint(x), __float_as_uint(x), false, false);
  return __uint_as_float(t[0]) + __uint_as_float(t[1]);
}
__device__ __forceinline__ float x16_max(float x) {
  auto s = __builtin_amdgcn_permlane16_swap(__float_as_uint(x), __float_as_uint(x), false, false);
  return fmaxf(__uint_as_float(s[0]), __uint_as_float(s[1]));
}
__device__ __forceinline__ float x32_max(float x) {
  auto t = __builtin_amdgcn_permlane32_swap(__float_as_uint(x), __float_as_uint(x), false, false);
  return fmaxf(__uint_as_float(t[0]), __uint_as_float(t[1]));
}
__device__ __forceinline__ float row_sum(float x) {
  x += dpp<XOR1>(x); x += dpp<XOR2>(x); x += dpp<XOR7>(x); x += dpp<XOR8>(x); return x;
}
__device__ __forceinline__ float row_max(float x) {
  x = fmaxf(x, dpp<XOR1>(x)); x = fmaxf(x, dpp<XOR2>(x)); x = fmaxf(x, dpp<XOR7>(x)); x = fmaxf(x, dpp<XOR8>(x)); return x;
}
__device__ __forceinline__ float tr_reduce16(float (&x)[16], int c) {
#define TR_STEP(HALF, CTRL, BIT) _Pragma("unroll") for (int hh = 0; hh < (HALF); ++hh) { \
    const float lo_ = x[hh], hi_ = x[hh + (HALF)]; const float t1_ = lo_ + dpp<CTRL>(lo_), t2_ = hi_ + dpp<CTRL>(hi_); \
    x[hh] = (c & (BIT)) ? t2_ : t1_; }
  TR_STEP(8, XOR8, 8) TR_STEP(4, XOR7, 4) TR_STEP(2, XOR2, 2) TR_STEP(1, XOR1, 1)
#undef TR_STEP
  return x[0];
}
__device__ __forceinline__ void put_granule(gu64* g, float v) {
  __hip_atomic_store(g, (1ull << 32) | (u64)__float_as_uint(v), RLX_AGENT);
}
__device__ __forceinline__ u64 epoch_mix(u64 e) { return e * 6364136223846793005ull + 1442695040888963407ull; }

constexpr int L_XS = 0;
constexpr int L_QP = 1280;
constexpr int L_QB = L_QP + 4096;
constexpr int L_UP = L_QB + 128;
constexpr int L_G = L_UP + 16 * 2 * FH;
constexpr int L_OC = L_G + 4;
constexpr int L_HC = L_OC + 256;
constexpr int L_END = L_HC + 128;

__global__ void __launch_bounds__(1024, 4)
fused_kernel(const u16* __restrict__ x, const u16* __restrict__ kc, const u16* __restrict__ vc,
             const u16* __restrict__ wq, const u16* __restrict__ wo, const u16* __restrict__ w1,
             const u16* __restrict__ w2, const u16* __restrict__ gw, u16* __restrict__ out, unsigned char* ws) {
  __shared__ __attribute__((aligned(16))) float lds[L_END];
  const int tid = threadIdx.x, lane = tid & 63;
  const int wave = __builtin_amdgcn_readfirstlane(tid >> 6);
  const int r = lane >> 4, c = lane & 15;
  const int bid = blockIdx.x, h = bid & 31, s = bid >> 5;
  const int b = wave >> 3, wv = wave & 7;

  gu64* boxes = (gu64*)(ws + WS_BOX);
  gu64* mybox = boxes + (size_t)bid * (BOX_BYTES / 8);
  if (tid < BOX_GRAN) __hip_atomic_store(mybox + tid, 0ull, RLX_AGENT);
  asm volatile("" ::: "memory");
  u64 tag = 0;
  if (wave == 0) tag = __hip_atomic_load((gu64*)(ws + WS_E), RLX_AGENT);

  u32x4 xr = {0u, 0u, 0u, 0u};
  if (tid < 160) xr = *reinterpret_cast<const u32x4*>(x + tid * 8);
  u32x4 wqr[10];
  {
    const u16* p = wq + (size_t)(40 * wave + r) * (NH * DH) + h * DH + 8 * c;
#pragma unroll
    for (int i = 0; i < 10; ++i) wqr[i] = *reinterpret_cast<const u32x4*>(p + (size_t)(4 * i) * (NH * DH));
  }
  u32 w1r[5] = {0u, 0u, 0u, 0u, 0u};
  if ((wave < 8 && (lane & 31) < 27) || wave == 15) {
    const u16* p = wave == 15 ? gw + 2 * lane : w1 + (size_t)(CS * s + 10 * wave + (lane >> 5)) * FF + FH * h + 2 * (lane & 31);
    const int stride = wave == 15 ? 128 : 2 * FF;
#pragma unroll
    for (int t = 0; t < 5; ++t) w1r[t] = *reinterpret_cast<const u32*>(p + (size_t)t * stride);
  }
  const size_t kvoff = ((size_t)(b * NH + h) * SK + s * (SK / NS) + wv * 64 + r) * DH + 8 * c;
  u32x4 K[16];
#pragma unroll
  for (int i = 0; i < KEARLY; ++i) K[i] = __builtin_nontemporal_load(reinterpret_cast<const u32x4*>(kc + kvoff + (size_t)(4 * i) * DH));

  if (tid < 160) {
    f32x4 a = {bflo(xr[0]), bfhi(xr[0]), bflo(xr[1]), bfhi(xr[1])};
    f32x4 bb = {bflo(xr[2]), bfhi(xr[2]), bflo(xr[3]), bfhi(xr[3])};
    *reinterpret_cast<f32x4*>(lds + L_XS + tid * 8) = a;
    *reinterpret_cast<f32x4*>(lds + L_XS + tid * 8 + 4) = bb;
  }
  __syncthreads();

  {
    float qa[2][8];
#pragma unroll
    for (int j = 0; j < 8; ++j) { qa[0][j] = 0.f; qa[1][j] = 0.f; }
#pragma unroll
    for (int i = 0; i < 10; ++i) {
      const int row = 40 * wave + 4 * i + r;
      const float x0 = lds[L_XS + row], x1 = lds[L_XS + DM + row];
#pragma unroll
      for (int k = 0; k < 4; ++k) {
        const float lo = bflo(wqr[i][k]), hi = bfhi(wqr[i][k]);
        qa[0][2 * k] = fmaf(x0, lo, qa[0][2 * k]); qa[0][2 * k + 1] = fmaf(x0, hi, qa[0][2 * k + 1]);
        qa[1][2 * k] = fmaf(x1, lo, qa[1][2 * k]); qa[1][2 * k + 1] = fmaf(x1, hi, qa[1][2 * k + 1]);
      }
    }
#pragma unroll
    for (int bb = 0; bb < 2; ++bb)
#pragma unroll
      for (int j = 0; j < 8; ++j) qa[bb][j] = x32_sum(x16_sum(qa[bb][j]));
    if (r < 2) {
      float* dst = lds + L_QP + (wave * 2 + r) * 128 + 8 * c;
      *reinterpret_cast<f32x4*>(dst) = r ? f32x4{qa[1][0], qa[1][1], qa[1][2], qa[1][3]} : f32x4{qa[0][0], qa[0][1], qa[0][2], qa[0][3]};
      *reinterpret_cast<f32x4*>(dst + 4) = r ? f32x4{qa[1][4], qa[1][5], qa[1][6], qa[1][7]} : f32x4{qa[0][4], qa[0][5], qa[0][6], qa[0][7]};
    }
  }
  __builtin_amdgcn_sched_barrier(0);
#pragma unroll
  for (int i = KEARLY; i < 16; ++i) K[i] = __builtin_nontemporal_load(reinterpret_cast<const u32x4*>(kc + kvoff + (size_t)(4 * i) * DH));
  __builtin_amdgcn_sched_barrier(0);
  if (wave < 8) {
    float u0 = 0.f, u1 = 0.f, u2 = 0.f, u3 = 0.f;
#pragma unroll
    for (int t = 0; t < 5; ++t) {
      const int row = CS * s + 10 * wave + 2 * t + (lane >> 5);
      const float x0 = lds[L_XS + row], x1 = lds[L_XS + DM + row];
      const float lo = bflo(w1r[t]), hi = bfhi(w1r[t]);
      u0 = fmaf(x0, lo, u0); u1 = fmaf(x0, hi, u1); u2 = fmaf(x1, lo, u2); u3 = fmaf(x1, hi, u3);
    }
    if ((lane & 31) < 27) {
      float* dst = lds + L_UP + (wave * 2 + (lane >> 5)) * (2 * FH) + 2 * (lane & 31);
      dst[0] = u0; dst[1] = u1; dst[FH] = u2; dst[FH + 1] = u3;
    }
  }
  if (wave == 15) {
    float g0 = 0.f, g1 = 0.f;
#pragma unroll
    for (int t = 0; t < 5; ++t) {
      const int d = 2 * lane + 128 * t;
      const float wl = bflo(w1r[t]), wh = bfhi(w1r[t]);
      g0 += bf16r(lds[L_XS + d] * wl); g1 += bf16r(lds[L_XS + DM + d] * wl);
      g0 += bf16r(lds[L_XS + d + 1] * wh); g1 += bf16r(lds[L_XS + DM + d + 1] * wh);
    }
    g0 = x32_sum(x16_sum(row_sum(g0))); g1 = x32_sum(x16_sum(row_sum(g1)));
    g0 = bf16r(g0); g1 = bf16r(g1);
    g0 = bf16r(1.f / (1.f + __expf(-g0))); g1 = bf16r(1.f / (1.f + __expf(-g1)));
    if (lane == 0) { lds[L_G] = g0; lds[L_G + 1] = g1; }
  }
  __syncthreads();
  asm volatile("" : "+v"(tag));
  tag = epoch_mix(tag);
  if (tid == 0) __hip_atomic_store((gu64*)(ws + WS_FLAG + (size_t)bid * 128), tag, RLX_AGENT);
  if (tid < 256) {
    float q = 0.f;
#pragma unroll
    for (int w = 0; w < 16; ++w) q += lds[L_QP + w * 256 + tid];
    reinterpret_cast<u16*>(lds + L_QB)[tid] = bf16bits(q);
  }
  __syncthreads();
  const u32x4 qp = *reinterpret_cast<const u32x4*>(lds + L_QB + b * 64 + 4 * c);

  u32x4 V[16];
  float sc[16];
#pragma unroll
  for (int i = 0; i < 16; ++i) {
    float d = 0.f;
#pragma unroll
    for (int k = 0; k < 4; ++k) d = dot2(K[i][k], qp[k], d);
    asm volatile("" : "+v"(d));
    sc[i] = d;
    V[i] = __builtin_nontemporal_load(reinterpret_cast<const u32x4*>(vc + kvoff + (size_t)(4 * i) * DH));
    __builtin_amdgcn_sched_barrier(0);
  }
  u32x4 wo0 = {0u, 0u, 0u, 0u}, wo1 = {0u, 0u, 0u, 0u}, w2r = {0u, 0u, 0u, 0u};
  if (wave < 10) {
    const u16* p = wo + (size_t)(h * DH + lane) * DM + CS * s + 8 * wave;
    wo0 = *reinterpret_cast<const u32x4*>(p);
    wo1 = *reinterpret_cast<const u32x4*>(p + (size_t)64 * DM);
    if (lane < FH) w2r = *reinterpret_cast<const u32x4*>(w2 + (size_t)(FH * h + lane) * DM + CS * s + 8 * wave);
  }
  gu64* fptr = (gu64*)(ws + WS_FLAG + (size_t)(lane < 8 ? lane * 32 + h : s * 32 + ((lane - 8) & 31)) * 128);
  u64 fl = 0;
  if (wave == 0) fl = __hip_atomic_load(fptr, RLX_AGENT);

  float sv = tr_reduce16(sc, c);
  sv = bf16r(bf16r(sv) * 0.08838834764831845f);
  const float mw = x32_max(x16_max(row_max(sv)));
  const float p = __builtin_amdgcn_exp2f((sv - mw) * 1.4426950408889634f);
  const float lw = x32_sum(x16_sum(row_sum(p)));
  float acc[8];
#pragma unroll
  for (int j = 0; j < 8; ++j) acc[j] = 0.f;
#define PV_STEP(I) { const float pi = dpp<0x150 + (I)>(p); \
    _Pragma("unroll") for (int k = 0; k < 4; ++k) { acc[2 * k] = fmaf(pi, bflo(V[I][k]), acc[2 * k]); acc[2 * k + 1] = fmaf(pi, bfhi(V[I][k]), acc[2 * k + 1]); } }
  PV_STEP(0) PV_STEP(1) PV_STEP(2) PV_STEP(3) PV_STEP(4) PV_STEP(5) PV_STEP(6) PV_STEP(7)
  PV_STEP(8) PV_STEP(9) PV_STEP(10) PV_STEP(11) PV_STEP(12) PV_STEP(13) PV_STEP(14) PV_STEP(15)
#undef PV_STEP
#pragma unroll
  for (int j = 0; j < 8; ++j) acc[j] = x32_sum(x16_sum(acc[j]));
  {
    float* rec = lds + L_QP + wave * 132;
    if (r == 0) {
      *reinterpret_cast<f32x4*>(rec + 8 * c) = f32x4{acc[0], acc[1], acc[2], acc[3]};
      *reinterpret_cast<f32x4*>(rec + 8 * c + 4) = f32x4{acc[4], acc[5], acc[6], acc[7]};
    }
    if (lane == 0) { rec[128] = mw; rec[129] = lw; }
  }
  if (wave == 0) {
    for (u32 spins = 0; spins < SPIN_CAP; ++spins) {
      if (__all(lane >= 40 || fl == tag)) break;
      __builtin_amdgcn_s_sleep(1);
      fl = __hip_atomic_load(fptr, RLX_AGENT);
    }
  }
  __syncthreads();

  if (tid < 256) {
    const int bb = tid >> 7, d = tid & 127;
    const float* rec = lds + L_QP + bb * 8 * 132;
    float M = rec[128];
#pragma unroll
    for (int w = 1; w < 8; ++w) M = fmaxf(M, rec[w * 132 + 128]);
    float L = 0.f, A = 0.f;
#pragma unroll
    for (int w = 0; w < 8; ++w) {
      const float e = __builtin_amdgcn_exp2f((rec[w * 132 + 128] - M) * 1.4426950408889634f);
      L = fmaf(e, rec[w * 132 + 129], L); A = fmaf(e, rec[w * 132 + d], A);
    }
    put_granule(mybox + bb * 130 + d, A);
    if (d < 2) put_granule(mybox + bb * 130 + 128 + d, d ? L : M);
  } else if (tid < 256 + 2 * FH) {
    const int i = tid - 256;
    float u = 0.f;
#pragma unroll
    for (int w = 0; w < 16; ++w) u += lds[L_UP + w * (2 * FH) + i];
    put_granule(mybox + 260 + i, u);
  }
  if (tid < 256) {
    const int bb = tid >> 7, d = tid & 127;
    float va[8], vm[8], vl[8];
    for (u32 spins = 0; spins < SPIN_CAP; ++spins) {
      bool ok = true;
#pragma unroll
      for (int sp = 0; sp < 8; ++sp) {
        gu64* bx = boxes + (size_t)(sp * 32 + h) * (BOX_BYTES / 8) + bb * 130;
        const u64 ga = __hip_atomic_load(bx + d, RLX_AGENT);
        const u64 gm = __hip_atomic_load(bx + 128, RLX_AGENT);
        const u64 gl = __hip_atomic_load(bx + 129, RLX_AGENT);
        va[sp] = __uint_as_float((u32)ga); vm[sp] = __uint_as_float((u32)gm); vl[sp] = __uint_as_float((u32)gl);
        ok &= ((ga & gm & gl) >> 32) == 1ull;
      }
      if (__all(ok)) break;
      __builtin_amdgcn_s_sleep(1);
    }
    float M = vm[0];
#pragma unroll
    for (int sp = 1; sp < 8; ++sp) M = fmaxf(M, vm[sp]);
    float L = 0.f, A = 0.f;
#pragma unroll
    for (int sp = 0; sp < 8; ++sp) {
      const float e = __builtin_amdgcn_exp2f((vm[sp] - M) * 1.4426950408889634f);
      L = fmaf(e, vl[sp], L); A = fmaf(e, va[sp], A);
    }
    lds[L_OC + tid] = bf16r(A / L) * lds[L_G + bb];
  } else if (tid < 256 + 128) {
    const int i = tid - 256, bb = i >> 6, f = i & 63;
    float hv = 0.f;
    if (f < FH) {
      float vu[8];
      for (u32 spins = 0; spins < SPIN_CAP; ++spins) {
        bool ok = true;
#pragma unroll
        for (int sp = 0; sp < 8; ++sp) {
          const u64 gu = __hip_atomic_load(boxes + (size_t)(sp * 32 + h) * (BOX_BYTES / 8) + 260 + bb * FH + f, RLX_AGENT);
          vu[sp] = __uint_as_float((u32)gu); ok &= (gu >> 32) == 1ull;
        }
        if (__all(ok)) break;
        __builtin_amdgcn_s_sleep(1);
      }
      float u = 0.f;
#pragma unroll
      for (int sp = 0; sp < 8; ++sp) u += vu[sp];
      u = bf16r(u);
      hv = bf16r(u / (1.f + __expf(-u))) * bf16r(1.f - lds[L_G + bb]);
    }
    lds[L_HC + i] = hv;
  }
  __syncthreads();

  if (wave < 10) {
    float y[16];
#pragma unroll
    for (int bb = 0; bb < 2; ++bb) {
      const float c0 = lds[L_OC + bb * 128 + lane], c1 = lds[L_OC + bb * 128 + 64 + lane], c2 = lds[L_HC + bb * 64 + lane];
#pragma unroll
      for (int k = 0; k < 4; ++k) {
        y[bb * 8 + 2 * k] = fmaf(c0, bflo(wo0[k]), fmaf(c1, bflo(wo1[k]), c2 * bflo(w2r[k])));
        y[bb * 8 + 2 * k + 1] = fmaf(c0, bfhi(wo0[k]), fmaf(c1, bfhi(wo1[k]), c2 * bfhi(w2r[k])));
      }
    }
    const float t = x32_sum(x16_sum(tr_reduce16(y, c)));
    if (r == 0) put_granule(mybox + B_OFF + (c >> 3) * CS + 8 * wave + (c & 7), t);
  }
  if (tid < 160) {
    const int hp = tid & 31, idx = 5 * h + (tid >> 5);
    gu64* g = boxes + (size_t)(s * 32 + hp) * (BOX_BYTES / 8) + B_OFF + idx;
    u64 gv = 0;
    for (u32 spins = 0; spins < SPIN_CAP; ++spins) {
      gv = __hip_atomic_load(g, RLX_AGENT);
      if (__all((gv >> 32) == 1ull)) break;
      __builtin_amdgcn_s_sleep(1);
    }
    const float t = x16_sum(row_sum(__uint_as_float((u32)gv)));
    if (hp == 0) { const int bb = idx / CS, col = idx - bb * CS; out[bb * DM + CS * s + col] = bf16bits(t); }
    if (bid == 0 && tid == 0) __hip_atomic_store((gu64*)(ws + WS_E), tag, RLX_AGENT);
  }
}

extern "C" void kernel_launch(void* const* d_in, const int* in_sizes, int n_in,
                              void* d_out, int out_size, void* d_ws, size_t ws_size,
                              hipStream_t stream) {
  const u16* x = (const u16*)d_in[0];
  const u16* kc = (const u16*)d_in[1];
  const u16* vc = (const u16*)d_in[2];
  const u16* wq = (const u16*)d_in[3];
  const u16* wo = (const u16*)d_in[4];
  const u16* w1 = (const u16*)d_in[6];
  const u16* w2 = (const u16*)d_in[7];
  const u16* gw = (const u16*)d_in[8];
  fused_kernel<<<256, 1024, 0, stream>>>(x, kc, vc, wq, wo, w1, w2, gw, (u16*)d_out, (unsigned char*)d_ws);
}
```

```cpp
#include <hip/hip_runtime.h>
#include <hip/hip_bf16.h>
#include <stdint.h>

typedef unsigned int u32;
typedef unsigned short u16;
typedef unsigned long long u64;
using u32x2 = __attribute__((ext_vector_type(2))) unsigned;
using u32x4 = __attribute__((ext_vector_type(4))) unsigned;
using f32x2 = __attribute__((ext_vector_type(2))) float;
using f32x4 = __attribute__((ext_vector_type(4))) float;
using bf16x2 = __attribute__((ext_vector_type(2))) __bf16;
#define GAS __attribute__((address_space(1)))
typedef GAS u64 gu64;
typedef GAS u32 gu32;
#define RLX_AGENT __ATOMIC_RELAXED, __HIP_MEMORY_SCOPE_AGENT

constexpr int DM = 640, NH = 32, SK = 4096, DH = 128, FF = 1728;
constexpr int NS = 8;
constexpr int FH = FF / NH;
constexpr int CS = DM / NS;
constexpr size_t WS_E = 0;
constexpr size_t WS_FLAG = 4096;
constexpr size_t WS_BOX = 65536;
constexpr int BX_Q = 0;
constexpr int BX_A = 256;
constexpr int BX_ML = 512;
constexpr int BX_U = 544;
constexpr int BX_B = 672;
constexpr int BX_WORDS = 832;
constexpr size_t BOX_BYTES = BX_WORDS * 4;
constexpr u32 SPIN_CAP = 1u << 18;
#define BSLEEP 1
#define STAMPW 0

__device__ __forceinline__ float dot2(u32 k, u32 q, float acc) {
  return __builtin_amdgcn_fdot2_f32_bf16(__builtin_bit_cast(bf16x2, k), __builtin_bit_cast(bf16x2, q), acc, false);
}
__device__ __forceinline__ float bflo(u32 v) { return __builtin_bit_cast(float, v << 16); }
__device__ __forceinline__ float bfhi(u32 v) { return __builtin_bit_cast(float, v & 0xffff0000u); }
__device__ __forceinline__ float bf16r(float x) { return (float)(__bf16)x; }
__device__ __forceinline__ u16 bf16bits(float x) { return __builtin_bit_cast(u16, (__bf16)x); }
template <int CTRL> __device__ __forceinline__ float dpp(float x) {
  return __builtin_bit_cast(float, __builtin_amdgcn_mov_dpp(__builtin_bit_cast(int, x), CTRL, 0xf, 0xf, true));
}
template <int CTRL> __device__ __forceinline__ u32 dppu(u32 x) {
  return (u32)__builtin_amdgcn_mov_dpp((int)x, CTRL, 0xf, 0xf, true);
}
constexpr int XOR1 = 0xB1, XOR2 = 0x4E, XOR7 = 0x141, XOR8 = 0x128;
__device__ __forceinline__ float sw16_add(float a, float b) {
  auto s = __builtin_amdgcn_permlane16_swap(__float_as_uint(a), __float_as_uint(b), false, false);
  return __uint_as_float(s[0]) + __uint_as_float(s[1]);
}
__device__ __forceinline__ float sw32_add(float a, float b) {
  auto t = __builtin_amdgcn_permlane32_swap(__float_as_uint(a), __float_as_uint(b), false, false);
  return __uint_as_float(t[0]) + __uint_as_float(t[1]);
}
__device__ __forceinline__ float x16_sum(float x) { return sw16_add(x, x); }
__device__ __forceinline__ float x32_sum(float x) { return sw32_add(x, x); }
__device__ __forceinline__ float x16_max(float x) {
  auto s = __builtin_amdgcn_permlane16_swap(__float_as_uint(x), __float_as_uint(x), false, false);
  return fmaxf(__uint_as_float(s[0]), __uint_as_float(s[1]));
}
__device__ __forceinline__ float x32_max(float x) {
  auto t = __builtin_amdgcn_permlane32_swap(__float_as_uint(x), __float_as_uint(x), false, false);
  return fmaxf(__uint_as_float(t[0]), __uint_as_float(t[1]));
}
__device__ __forceinline__ float row_sum(float x) {
  x += dpp<XOR1>(x); x += dpp<XOR2>(x); x += dpp<XOR7>(x); x += dpp<XOR8>(x); return x;
}
__device__ __forceinline__ float row_max(float x) {
  x = fmaxf(x, dpp<XOR1>(x)); x = fmaxf(x, dpp<XOR2>(x)); x = fmaxf(x, dpp<XOR7>(x)); x = fmaxf(x, dpp<XOR8>(x)); return x;
}
__device__ __forceinline__ float tr_reduce16(float (&x)[16], int c) {
#define TR_STEP(HALF, CTRL, BIT) _Pragma("unroll") for (int hh = 0; hh < (HALF); ++hh) { \
    const float lo_ = x[hh], hi_ = x[hh + (HALF)]; const float t1_ = lo_ + dpp<CTRL>(lo_), t2_ = hi_ + dpp<CTRL>(hi_); \
    x[hh] = (c & (BIT)) ? t2_ : t1_; }
  TR_STEP(8, XOR8, 8) TR_STEP(4, XOR7, 4) TR_STEP(2, XOR2, 2) TR_STEP(1, XOR1, 1)
#undef TR_STEP
  return x[0];
}
__device__ __forceinline__ void put_word(gu32* g, float v) { __hip_atomic_store(g, __float_as_uint(v) | 1u, RLX_AGENT); }
__device__ __forceinline__ void put_word2(gu32* g, float v0, float v1) {
  __hip_atomic_store((gu64*)g, (u64)(__float_as_uint(v0) | 1u) | ((u64)(__float_as_uint(v1) | 1u) << 32), RLX_AGENT);
}
__device__ __forceinline__ u32x4 get_word4(const gu32* g) {
  u32x4 v;
  asm volatile("global_load_dwordx4 %0, %1, off sc1\n\ts_waitcnt vmcnt(0)" : "=&v"(v) : "v"(g) : "memory");
  return v;
}
__device__ __forceinline__ u64 epoch_mix(u64 e) { return e * 6364136223846793005ull + 1442695040888963407ull; }

#define STAMP(k) do {} while (0)
constexpr int L_QB = 0;
constexpr int L_REC = 128;
constexpr int L_OB = L_REC + 1024;
constexpr int L_ML = L_OB + 192;
constexpr int L_F = L_ML + 16;
constexpr int L_G = L_F + 8;
constexpr int L_CNT = L_G + 2;
constexpr int L_UP = L_CNT + 6;
constexpr int L_WT = L_UP + 1024;
constexpr int WT_STRIDE = 44;
constexpr int L_END = L_WT + 192 * WT_STRIDE;
typedef short s16x4 __attribute__((ext_vector_type(4)));
using bf16x8 = __attribute__((ext_vector_type(8))) __bf16;
#define RNG 24
#define WIN 4
#define DMIN (WIN + 2)
constexpr int niss(int i) { const int a = i + 1 + DMIN, b = RNG + (i + 1) / 3; const int m = a > b ? a : b; return m > 64 ? 64 : m; }
#define LAS __attribute__((address_space(3)))
__device__ __forceinline__ u32x4 ldnt(const u16* p) { return __builtin_nontemporal_load(reinterpret_cast<const u32x4*>(p)); }
__device__ __forceinline__ u32 ldnt1(const u16* p) { return __builtin_nontemporal_load(reinterpret_cast<const u32*>(p)); }
template <int N> __device__ __forceinline__ void wait_vm() { asm volatile("s_waitcnt vmcnt(%0)" :: "i"(N) : "memory"); }
__device__ __forceinline__ void lds_wait(float* lds, int idx, u32 want) {
  for (u32 spins = 0; spins < (1u << 22); ++spins) {
    if (__builtin_amdgcn_readfirstlane(__hip_atomic_load((LAS u32*)(lds + idx), __ATOMIC_ACQUIRE, __HIP_MEMORY_SCOPE_WORKGROUP)) >= want) break;
    __builtin_amdgcn_s_sleep(1);
  }
}
__device__ __forceinline__ void lds_arrive(float* lds, int idx, int lane) {
  if (lane == 0) __hip_atomic_fetch_add((LAS u32*)(lds + idx), 1u, __ATOMIC_RELEASE, __HIP_MEMORY_SCOPE_WORKGROUP);
}
__device__ __forceinline__ u32 all4(u32x4 v) { return v[0] & v[1] & v[2] & v[3]; }

__global__ void __launch_bounds__(576, 3)
fused_kernel(const u16* __restrict__ x, const u16* __restrict__ kc, const u16* __restrict__ vc,
             const u16* __restrict__ wq, const u16* __restrict__ wo, const u16* __restrict__ w1,
             const u16* __restrict__ w2, const u16* __restrict__ gw, u16* __restrict__ out, unsigned char* ws) {
  __shared__ __attribute__((aligned(16))) float lds[L_END];
  const int tid = threadIdx.x, lane = tid & 63;
  const int wave = __builtin_amdgcn_readfirstlane(tid >> 6);
  const int r = lane >> 4, c = lane & 15;
#define HMAP 1
  const int s = HMAP ? (blockIdx.x >> 3) & 7 : blockIdx.x >> 5;
  const int h = HMAP ? (blockIdx.x >> 6) * 8 + ((blockIdx.x + s + (blockIdx.x >> 6)) & 7) : blockIdx.x & 31;
  const int bid = s * 32 + h;
  STAMP(0);
  if (tid < 4) reinterpret_cast<u32*>(lds)[L_CNT + tid] = 0u;
  gu32* boxes = (gu32*)(ws + WS_BOX);
  gu32* mybox = boxes + (size_t)bid * BX_WORDS;
  u64 tag = 0;

  if (wave < 8) {
    const int b = wave >> 2, wv = wave & 3;
    const size_t kvoff = ((size_t)(b * NH + h) * SK + s * (SK / NS) + 4 * wv + r) * DH + 8 * c;
    const u16* kp = kc + kvoff;
    const u16* vp = vc + kvoff;
    u32x4 ring[RNG];
    __syncthreads();
    const int stg = tid < 480 ? tid : 479;
    const int srg = stg / 10, sch = stg - 10 * srg;
    u32x4 wst[4];
#pragma unroll
    for (int j = 0; j < 4; ++j) {
      const int row = min(srg + 48 * j, 181);
      const u16* p = row < DH ? wo + (size_t)(h * DH + row) * DM : w2 + (size_t)(FH * h + row - DH) * DM;
      wst[j] = ldnt(p + CS * s + 8 * sch);
    }
    __builtin_amdgcn_sched_barrier(0);
    u32x4 w1r[2];
    u32 xm[2][2];
    {
      const int col0 = min((FH * h) & ~7, FF - 64) + 8 * (lane & 7);
#pragma unroll
      for (int t = 0; t < 2; ++t) {
        const int rr = (lane >> 3) + 8 * t, row = CS * s + 10 * wave + (rr < 10 ? rr : 9);
        wait_vm<WIN - 1>();
        w1r[t] = ldnt(w1 + (size_t)row * FF + col0);
        xm[t][0] = x[row]; xm[t][1] = x[DM + row];
        __builtin_amdgcn_sched_barrier(0);
      }
    }
    __builtin_amdgcn_sched_barrier(0);
#define ISSUE(N) { wait_vm<WIN - 1>(); \
      if ((N) < 32) ring[(N) % RNG] = ldnt(kp + (size_t)(16 * (N)) * DH); else ring[(N) % RNG] = ldnt(vp + (size_t)(16 * ((N) - 32)) * DH); \
      __builtin_amdgcn_sched_barrier(0); }
#pragma unroll
    for (int i = 0; i < RNG; ++i) ISSUE(i)
    STAMP(1);
    {
#pragma unroll
      for (int j = 0; j < 4; ++j) {
        const int row = srg + 48 * j;
        *reinterpret_cast<u32x4*>(lds + L_WT + row * WT_STRIDE + 4 * sch) = row < 182 ? wst[j] : u32x4{0u, 0u, 0u, 0u};
      }
      float u[16];
      const float m1 = lane < 16 ? 1.f : 0.f;
#pragma unroll
      for (int bb = 0; bb < 2; ++bb) {
        const float x0 = bflo(xm[0][bb]), x1 = bflo(xm[1][bb]) * m1;
#pragma unroll
        for (int k = 0; k < 4; ++k) {
          u[bb * 8 + 2 * k] = fmaf(x0, bflo(w1r[0][k]), x1 * bflo(w1r[1][k]));
          u[bb * 8 + 2 * k + 1] = fmaf(x0, bfhi(w1r[0][k]), x1 * bfhi(w1r[1][k]));
        }
      }
      float v8[8], v4[4];
#pragma unroll
      for (int i = 0; i < 16; ++i) u[i] += dpp<XOR8>(u[i]);
#pragma unroll
      for (int i = 0; i < 8; ++i) v8[i] = sw16_add(u[i], u[i + 8]);
#pragma unroll
      for (int i = 0; i < 4; ++i) v4[i] = sw32_add(v8[i], v8[i + 4]);
      if (c < 8) {
        float* dst = lds + L_UP + wave * 128 + 2 * (8 * c + 4 * (r >> 1)) + (r & 1);
#pragma unroll
        for (int i = 0; i < 4; ++i) dst[2 * i] = v4[i];
      }
      lds_arrive(lds, L_CNT + 3, lane);
    }
    lds_wait(lds, L_CNT, 1);
    const u32x4 qp = *reinterpret_cast<const u32x4*>(lds + L_QB + b * 64 + 4 * c);
    STAMP(2);
    float sc[2][16];
#pragma unroll
    for (int i = 0; i < 32; ++i) {
      float d = 0.f;
#pragma unroll
      for (int k = 0; k < 4; ++k) d = dot2(ring[i % RNG][k], qp[k], d);
      asm volatile("" : "+v"(d));
      sc[i >> 4][i & 15] = d;
#pragma unroll
      for (int n = niss(i - 1); n < niss(i); ++n) ISSUE(n)
    }
    STAMP(3);
    float p0, p1;
    {
      float s0 = tr_reduce16(sc[0], c), s1 = tr_reduce16(sc[1], c);
      s0 = bf16r(bf16r(s0) * 0.08838834764831845f);
      s1 = bf16r(bf16r(s1) * 0.08838834764831845f);
      const float mw = x32_max(x16_max(row_max(fmaxf(s0, s1))));
      p0 = __builtin_amdgcn_exp2f((s0 - mw) * 1.4426950408889634f);
      p1 = __builtin_amdgcn_exp2f((s1 - mw) * 1.4426950408889634f);
      const float lw = x32_sum(x16_sum(row_sum(p0 + p1)));
      if (lane == 0) *reinterpret_cast<f32x2*>(lds + L_ML + 2 * wave) = f32x2{mw, lw};
      lds_arrive(lds, L_CNT + 1, lane);
    }
    float acc[8];
#pragma unroll
    for (int j = 0; j < 8; ++j) acc[j] = 0.f;
#define PV_STEP(I) { const float pi = dpp<0x150 + ((I) & 15)>((I) < 16 ? p0 : p1); const u32x4 v = ring[(32 + (I)) % RNG]; \
      _Pragma("unroll") for (int k = 0; k < 4; ++k) { acc[2 * k] = fmaf(pi, bflo(v[k]), acc[2 * k]); acc[2 * k + 1] = fmaf(pi, bfhi(v[k]), acc[2 * k + 1]); } \
      asm volatile("" : "+v"(acc[0]), "+v"(acc[1]), "+v"(acc[2]), "+v"(acc[3]), "+v"(acc[4]), "+v"(acc[5]), "+v"(acc[6]), "+v"(acc[7])); \
      _Pragma("unroll") for (int n = niss(31 + (I)); n < niss(32 + (I)); ++n) ISSUE(n) }
    PV_STEP(0) PV_STEP(1) PV_STEP(2) PV_STEP(3) PV_STEP(4) PV_STEP(5) PV_STEP(6) PV_STEP(7)
    PV_STEP(8) PV_STEP(9) PV_STEP(10) PV_STEP(11) PV_STEP(12) PV_STEP(13) PV_STEP(14) PV_STEP(15)
    PV_STEP(16) PV_STEP(17) PV_STEP(18) PV_STEP(19) PV_STEP(20) PV_STEP(21) PV_STEP(22) PV_STEP(23)
    PV_STEP(24) PV_STEP(25) PV_STEP(26) PV_STEP(27) PV_STEP(28) PV_STEP(29) PV_STEP(30) PV_STEP(31)
#undef PV_STEP
#undef ISSUE
    {
      float u[4], t[2];
#pragma unroll
      for (int i = 0; i < 4; ++i) u[i] = sw16_add(acc[i], acc[i + 4]);
#pragma unroll
      for (int i = 0; i < 2; ++i) t[i] = sw32_add(u[i], u[i + 2]);
      lds_wait(lds, L_CNT + 2, 1);
      const float f = lds[L_F + wave];
      *reinterpret_cast<f32x2*>(lds + L_REC + wave * 128 + 8 * c + 4 * (r & 1) + 2 * (r >> 1)) = f32x2{t[0] * f, t[1] * f};
    }
  } else {
#pragma unroll
    for (int i = 0; i < 7; ++i) __hip_atomic_store((gu64*)mybox + min(lane + 64 * i, BX_WORDS / 2 - 1), 0ull, RLX_AGENT);
    asm volatile("" ::: "memory");
    tag = __hip_atomic_load((gu64*)(ws + WS_E), RLX_AGENT);
    u32 xq0, xq1;
    u64 fl0;
    {
      const u16* p = x + CS * s + 8 * (c < 10 ? c : 9) + 2 * r;
      xq0 = *reinterpret_cast<const u32*>(p); xq1 = *reinterpret_cast<const u32*>(p + DM);
    }
    {
      u32x4 wqr[20];
      const u16* p = wq + (size_t)(CS * s + 2 * r) * (NH * DH) + h * DH + 8 * c;
#pragma unroll
      for (int j = 0; j < 10; ++j) {
        wqr[2 * j] = ldnt(p + (size_t)(8 * j) * (NH * DH));
        wqr[2 * j + 1] = ldnt(p + (size_t)(8 * j + 1) * (NH * DH));
      }
      __builtin_amdgcn_sched_barrier(0);
      __builtin_amdgcn_s_barrier();
      __builtin_amdgcn_sched_barrier(0);
      wait_vm<22>();
      asm volatile("" : "+v"(tag));
      tag = epoch_mix(tag);
      __hip_atomic_store((gu64*)(ws + WS_FLAG + (size_t)bid * 128), tag, RLX_AGENT);
      __builtin_amdgcn_sched_barrier(0);
      fl0 = __hip_atomic_load((gu64*)(ws + WS_FLAG + (size_t)((lane & 7) * 32 + h) * 128), RLX_AGENT);
      __builtin_amdgcn_sched_barrier(0);
      float qa[16];
#pragma unroll
      for (int j = 0; j < 16; ++j) qa[j] = 0.f;
#define Q_STEP(J) { const u32 x0 = dppu<0x150 + (J)>(xq0), x1 = dppu<0x150 + (J)>(xq1); \
      _Pragma("unroll") for (int k = 0; k < 4; ++k) { \
        const u32 lo = __builtin_amdgcn_perm(wqr[2 * (J) + 1][k], wqr[2 * (J)][k], 0x05040100u); \
        const u32 hi = __builtin_amdgcn_perm(wqr[2 * (J) + 1][k], wqr[2 * (J)][k], 0x07060302u); \
        qa[2 * k] = dot2(lo, x0, qa[2 * k]); qa[2 * k + 1] = dot2(hi, x0, qa[2 * k + 1]); \
        qa[8 + 2 * k] = dot2(lo, x1, qa[8 + 2 * k]); qa[8 + 2 * k + 1] = dot2(hi, x1, qa[8 + 2 * k + 1]); } }
      Q_STEP(0) Q_STEP(1) Q_STEP(2) Q_STEP(3) Q_STEP(4) Q_STEP(5) Q_STEP(6) Q_STEP(7) Q_STEP(8) Q_STEP(9)
#undef Q_STEP
      float u[8], t[4];
#pragma unroll
      for (int i = 0; i < 8; ++i) u[i] = sw16_add(qa[i], qa[i + 8]);
#pragma unroll
      for (int i = 0; i < 4; ++i) t[i] = sw32_add(u[i], u[i + 4]);
      gu32* dst = mybox + BX_Q + (r & 1) * 128 + 8 * c + 4 * (r >> 1);
      put_word2(dst, t[0], t[1]); put_word2(dst + 2, t[2], t[3]);
    }
    STAMP(1);
    gu64* fptr = (gu64*)(ws + WS_FLAG + (size_t)(lane < 8 ? lane * 32 + h : s * 32 + ((lane - 8) & 31)) * 128);
    {
      const gu32* qsrc = boxes + (size_t)h * BX_WORDS + BX_Q + (r & 1) * 128 + 8 * c + 4 * (r >> 1);
      constexpr size_t GS = (size_t)32 * BX_WORDS;
      u32x4 g[8];
      bool flags_ok = false;
      for (u32 spins = 0; spins < SPIN_CAP; ++spins) {
        u64 fl;
        asm volatile("global_load_dwordx2 %8, %17, off sc1\n\t"
                     "global_load_dwordx4 %0, %9, off sc1\n\tglobal_load_dwordx4 %1, %10, off sc1\n\t"
                     "global_load_dwordx4 %2, %11, off sc1\n\tglobal_load_dwordx4 %3, %12, off sc1\n\t"
                     "global_load_dwordx4 %4, %13, off sc1\n\tglobal_load_dwordx4 %5, %14, off sc1\n\t"
                     "global_load_dwordx4 %6, %15, off sc1\n\tglobal_load_dwordx4 %7, %16, off sc1\n\ts_waitcnt vmcnt(0)"
                     : "=&v"(g[0]), "=&v"(g[1]), "=&v"(g[2]), "=&v"(g[3]), "=&v"(g[4]), "=&v"(g[5]), "=&v"(g[6]), "=&v"(g[7]), "=&v"(fl)
                     : "v"(qsrc), "v"(qsrc + GS), "v"(qsrc + 2 * GS), "v"(qsrc + 3 * GS), "v"(qsrc + 4 * GS), "v"(qsrc + 5 * GS), "v"(qsrc + 6 * GS), "v"(qsrc + 7 * GS), "v"(fptr)
                     : "memory");
        flags_ok = flags_ok || __all(lane >= 8 || fl0 == tag);
        const u32 ok = all4(g[0]) & all4(g[1]) & all4(g[2]) & all4(g[3]) & all4(g[4]) & all4(g[5]) & all4(g[6]) & all4(g[7]);
        if (flags_ok && __all(ok & 1u)) break;
        fl0 = fl;
      }
      STAMP(2);
      f32x4 q = {0.f, 0.f, 0.f, 0.f};
#pragma unroll
      for (int sp = 0; sp < 8; ++sp) q += __builtin_bit_cast(f32x4, g[sp]);
      u32x2 pk = {(u32)bf16bits(q[0]) | ((u32)bf16bits(q[1]) << 16), (u32)bf16bits(q[2]) | ((u32)bf16bits(q[3]) << 16)};
      *reinterpret_cast<u32x2*>(lds + L_QB + (r & 1) * 64 + 4 * c + 2 * (r >> 1)) = pk;
      lds_arrive(lds, L_CNT, lane);
    }
    STAMP(3);
    lds_wait(lds, L_CNT + 3, 8);
    {
      const int cw = FH * h - min((FH * h) & ~7, FF - 64) + (lane < FH ? lane : 0);
      f32x2 u = {0.f, 0.f};
#pragma unroll
      for (int w = 0; w < 8; ++w) u += *reinterpret_cast<const f32x2*>(lds + L_UP + w * 128 + 2 * cw);
      if (lane < FH) put_word2(mybox + BX_U + 2 * lane, u[0], u[1]);
    }
    {
      float g0 = 0.f, g1 = 0.f;
#pragma unroll
      for (int t = 0; t < 5; ++t) {
        const u32 w = *reinterpret_cast<const u32*>(gw + 2 * lane + 128 * t);
        const u32 xa = *reinterpret_cast<const u32*>(x + 2 * lane + 128 * t), xb = *reinterpret_cast<const u32*>(x + DM + 2 * lane + 128 * t);
        g0 += bf16r(bflo(xa) * bflo(w)); g1 += bf16r(bflo(xb) * bflo(w));
        g0 += bf16r(bfhi(xa) * bfhi(w)); g1 += bf16r(bfhi(xb) * bfhi(w));
      }
      g0 = x32_sum(x16_sum(row_sum(g0))); g1 = x32_sum(x16_sum(row_sum(g1)));
      g0 = bf16r(g0); g1 = bf16r(g1);
      g0 = bf16r(1.f / (1.f + __expf(-g0))); g1 = bf16r(1.f / (1.f + __expf(-g1)));
      if (lane == 0) { lds[L_G] = g0; lds[L_G + 1] = g1; }
      STAMP(4);
      lds_wait(lds, L_CNT + 1, 8);
      const int wl = lane & 7;
      const f32x2 ml = *reinterpret_cast<const f32x2*>(lds + L_ML + 2 * wl);
      float M = fmaxf(ml[0], dpp<XOR1>(ml[0])); M = fmaxf(M, dpp<XOR2>(M));
      float L = ml[1] * __builtin_amdgcn_exp2f((ml[0] - M) * 1.4426950408889634f);
      L += dpp<XOR1>(L); L += dpp<XOR2>(L);
      if (lane == 0 || lane == 4) put_word2(mybox + BX_ML + (lane >> 1), M, L);
      const gu32* msrc = boxes + (size_t)(wl * 32 + h) * BX_WORDS + BX_ML;
      u32x4 gm;
      for (u32 spins = 0; spins < SPIN_CAP; ++spins) {
        gm = get_word4(msrc);
        if (__all(all4(gm) & 1u)) break;
        __builtin_amdgcn_s_sleep(2);
      }
      float fsc[2];
#pragma unroll
      for (int bb = 0; bb < 2; ++bb) {
        const float Ms = __uint_as_float(gm[2 * bb]), Ls = __uint_as_float(gm[2 * bb + 1]);
        float Mg = fmaxf(Ms, dpp<XOR1>(Ms)); Mg = fmaxf(Mg, dpp<XOR2>(Mg)); Mg = fmaxf(Mg, dpp<XOR7>(Mg));
        float Lg = Ls * __builtin_amdgcn_exp2f((Ms - Mg) * 1.4426950408889634f);
        Lg += dpp<XOR1>(Lg); Lg += dpp<XOR2>(Lg); Lg += dpp<XOR7>(Lg);
        fsc[bb] = __builtin_amdgcn_exp2f((ml[0] - Mg) * 1.4426950408889634f) / Lg;
      }
      if (lane < 8) lds[L_F + lane] = lane < 4 ? fsc[0] : fsc[1];
      lds_arrive(lds, L_CNT + 2, lane);
      u64 ug[8];
      for (u32 spins = 0; spins < SPIN_CAP; ++spins) {
        u64 all = ~0ull;
#pragma unroll
        for (int sp = 0; sp < 8; ++sp) {
          ug[sp] = __hip_atomic_load((gu64*)(boxes + (size_t)(sp * 32 + h) * BX_WORDS + BX_U) + (lane < FH ? lane : 0), RLX_AGENT);
          all &= ug[sp];
        }
        if (__all((all & 0x100000001ull) == 0x100000001ull)) break;
        __builtin_amdgcn_s_sleep(2);
      }
      float u0 = 0.f, u1 = 0.f;
#pragma unroll
      for (int sp = 0; sp < 8; ++sp) { u0 += __uint_as_float((u32)ug[sp]); u1 += __uint_as_float((u32)(ug[sp] >> 32)); }
      u0 = bf16r(u0); u1 = bf16r(u1);
      u16* ob = reinterpret_cast<u16*>(lds + L_OB);
      ob[DH + lane] = lane < FH ? bf16bits(u0 / (1.f + __expf(-u0))) : (u16)0;
      ob[192 + DH + lane] = lane < FH ? bf16bits(u1 / (1.f + __expf(-u1))) : (u16)0;
    }
    for (u32 spins = 0; spins < SPIN_CAP; ++spins) {
      const u64 fl = __hip_atomic_load(fptr, RLX_AGENT);
      if (__all(lane >= 40 || fl == tag)) break;
      __builtin_amdgcn_s_sleep(2);
    }
  }
  if (wave < 8) STAMP(4);
  __syncthreads();
  STAMP(5);

  if (wave < 4) {
    const float* rec = lds + L_REC + (tid >> 7) * 512 + (tid & 127);
    put_word(mybox + BX_A + tid, (rec[0] + rec[128]) + (rec[256] + rec[384]));
  }
  u32x4 wg[6];
  {
    float* wsrc = lds + L_WT + (8 * r + (c >> 2)) * WT_STRIDE + 8 * (wave < 5 ? wave : 4) + 2 * (c & 3);
#pragma unroll
    for (int ks = 0; ks < 6; ++ks) {
      const s16x4 lo = __builtin_amdgcn_ds_read_tr16_b64_v4i16((LAS s16x4*)(wsrc + (32 * ks) * WT_STRIDE));
      const s16x4 hi = __builtin_amdgcn_ds_read_tr16_b64_v4i16((LAS s16x4*)(wsrc + (32 * ks + 4) * WT_STRIDE));
      const u32x2 l2 = __builtin_bit_cast(u32x2, lo), h2 = __builtin_bit_cast(u32x2, hi);
      wg[ks] = u32x4{l2[0], l2[1], h2[0], h2[1]};
    }
  }
  if (wave < 8) {
    const gu32* src = boxes + (size_t)((lane >> 3) * 32 + h) * BX_WORDS + BX_A + 32 * wave + 4 * (lane & 7);
    u32x4 ga;
    for (u32 spins = 0; spins < SPIN_CAP; ++spins) {
      ga = get_word4(src);
      if (__all(all4(ga) & 1u)) break;
    }
    float o[4];
#pragma unroll
    for (int k = 0; k < 4; ++k) { float t = __uint_as_float(ga[k]); t += dpp<XOR8>(t); o[k] = x32_sum(x16_sum(t)); }
    if (lane < 8) {
      u32x2 pk = {(u32)bf16bits(o[0]) | ((u32)bf16bits(o[1]) << 16), (u32)bf16bits(o[2]) | ((u32)bf16bits(o[3]) << 16)};
      *reinterpret_cast<u32x2*>(lds + L_OB + (wave >> 2) * 96 + 16 * (wave & 3) + 2 * lane) = pk;
    }
  }
  __syncthreads();
  STAMP(6);

  if (wave < 5) {
    f32x4 ya = {0.f, 0.f, 0.f, 0.f}, yb = {0.f, 0.f, 0.f, 0.f};
    const float* arow = lds + L_OB + (c & 1) * 96 + 4 * r;
#pragma unroll
    for (int ks = 0; ks < 6; ++ks) {
      u32x4 af = *reinterpret_cast<const u32x4*>(arow + 16 * ks);
      if (c >= 2) af = u32x4{0u, 0u, 0u, 0u};
      if (ks < 4) ya = __builtin_amdgcn_mfma_f32_16x16x32_bf16(__builtin_bit_cast(bf16x8, af), __builtin_bit_cast(bf16x8, wg[ks]), ya, 0, 0, 0);
      else yb = __builtin_amdgcn_mfma_f32_16x16x32_bf16(__builtin_bit_cast(bf16x8, af), __builtin_bit_cast(bf16x8, wg[ks]), yb, 0, 0, 0);
    }
    if (lane < 16) {
      const float g0 = lds[L_G], g1 = lds[L_G + 1];
      put_word(mybox + BX_B + 16 * wave + lane, fmaf(g0, ya[0], bf16r(1.f - g0) * yb[0]));
      put_word(mybox + BX_B + CS + 16 * wave + lane, fmaf(g1, ya[1], bf16r(1.f - g1) * yb[1]));
    }
  }
  STAMP(7);
  if (tid < 160) {
    const int hp = tid & 31, idx = 5 * h + (tid >> 5);
    gu32* g = boxes + (size_t)(s * 32 + hp) * BX_WORDS + BX_B + idx;
    u32 gv = 0;
    for (u32 spins = 0; spins < SPIN_CAP; ++spins) {
      gv = __hip_atomic_load(g, RLX_AGENT);
      if (__all(gv & 1u)) break;
      __builtin_amdgcn_s_sleep(BSLEEP);
    }
    const float t = x16_sum(row_sum(__uint_as_float(gv)));
    if (hp == 0) { const int bb = idx / CS, col = idx - bb * CS; out[bb * DM + CS * s + col] = bf16bits(t); }
  }
  if (bid == 0) {
    __syncthreads();
    if (tid == 512) __hip_atomic_store((gu64*)(ws + WS_E), tag, RLX_AGENT);
  }
}

extern "C" void kernel_launch(void* const* d_in, const int* in_sizes, int n_in,
                              void* d_out, int out_size, void* d_ws, size_t ws_size,
                              hipStream_t stream) {
  const u16* x = (const u16*)d_in[0];
  const u16* kc = (const u16*)d_in[1];
  const u16* vc = (const u16*)d_in[2];
  const u16* wq = (const u16*)d_in[3];
  const u16* wo = (const u16*)d_in[4];
  const u16* w1 = (const u16*)d_in[6];
  const u16* w2 = (const u16*)d_in[7];
  const u16* gw = (const u16*)d_in[8];
  fused_kernel<<<256, 576, 0, stream>>>(x, kc, vc, wq, wo, w1, w2, gw, (u16*)d_out, (unsigned char*)d_ws);
}
```

```cpp
#include <hip/hip_runtime.h>
#include <hip/hip_bf16.h>
#include <stdint.h>

typedef unsigned int u32;
typedef unsigned short u16;
typedef unsigned long long u64;
using u32x2 = __attribute__((ext_vector_type(2))) unsigned;
using u32x4 = __attribute__((ext_vector_type(4))) unsigned;
using f32x2 = __attribute__((ext_vector_type(2))) float;
using f32x4 = __attribute__((ext_vector_type(4))) float;
using bf16x2 = __attribute__((ext_vector_type(2))) __bf16;
#define GAS __attribute__((address_space(1)))
typedef GAS u64 gu64;
typedef GAS u32 gu32;
#define RLX_AGENT __ATOMIC_RELAXED, __HIP_MEMORY_SCOPE_AGENT

constexpr int DM = 640, NH = 32, SK = 4096, DH = 128, FF = 1728;
constexpr int NS = 8;
constexpr int FH = FF / NH;
constexpr int CS = DM / NS;
constexpr size_t WS_E = 0;
constexpr size_t WS_FLAG = 4096;
constexpr size_t WS_BOX = 65536;
constexpr int BX_Q = 0;
constexpr int BX_A = 256;
constexpr int BX_ML = 512;
constexpr int BX_U = 544;
constexpr int BX_B = 672;
constexpr int BX_WORDS = 832;
constexpr size_t BOX_BYTES = BX_WORDS * 4;
constexpr u32 SPIN_CAP = 1u << 18;
#define BSLEEP 1
#define STAMPW 0

__device__ __forceinline__ float dot2(u32 k, u32 q, float acc) {
  return __builtin_amdgcn_fdot2_f32_bf16(__builtin_bit_cast(bf16x2, k), __builtin_bit_cast(bf16x2, q), acc, false);
}
__device__ __forceinline__ float bflo(u32 v) { return __builtin_bit_cast(float, v << 16); }
__device__ __forceinline__ float bfhi(u32 v) { return __builtin_bit_cast(float, v & 0xffff0000u); }
__device__ __forceinline__ float bf16r(float x) { return (float)(__bf16)x; }
__device__ __forceinline__ u16 bf16bits(float x) { return __builtin_bit_cast(u16, (__bf16)x); }
template <int CTRL> __device__ __forceinline__ float dpp(float x) {
  return __builtin_bit_cast(float, __builtin_amdgcn_mov_dpp(__builtin_bit_cast(int, x), CTRL, 0xf, 0xf, true));
}
template <int CTRL> __device__ __forceinline__ u32 dppu(u32 x) {
  return (u32)__builtin_amdgcn_mov_dpp((int)x, CTRL, 0xf, 0xf, true);
}
constexpr int XOR1 = 0xB1, XOR2 = 0x4E, XOR7 = 0x141, XOR8 = 0x128;
__device__ __forceinline__ float sw16_add(float a, float b) {
  auto s = __builtin_amdgcn_permlane16_swap(__float_as_uint(a), __float_as_uint(b), false, false);
  return __uint_as_float(s[0]) + __uint_as_float(s[1]);
}
__device__ __forceinline__ float sw32_add(float a, float b) {
  auto t = __builtin_amdgcn_permlane32_swap(__float_as_uint(a), __float_as_uint(b), false, false);
  return __uint_as_float(t[0]) + __uint_as_float(t[1]);
}
__device__ __forceinline__ float x16_sum(float x) { return sw16_add(x, x); }
__device__ __forceinline__ float x32_sum(float x) { return sw32_add(x, x); }
__device__ __forceinline__ float x16_max(float x) {
  auto s = __builtin_amdgcn_permlane16_swap(__float_as_uint(x), __float_as_uint(x), false, false);
  return fmaxf(__uint_as_float(s[0]), __uint_as_float(s[1]));
}
__device__ __forceinline__ float x32_max(float x) {
  auto t = __builtin_amdgcn_permlane32_swap(__float_as_uint(x), __float_as_uint(x), false, false);
  return fmaxf(__uint_as_float(t[0]), __uint_as_float(t[1]));
}
__device__ __forceinline__ float row_sum(float x) {
  x += dpp<XOR1>(x); x += dpp<XOR2>(x); x += dpp<XOR7>(x); x += dpp<XOR8>(x); return x;
}
__device__ __forceinline__ float row_max(float x) {
  x = fmaxf(x, dpp<XOR1>(x)); x = fmaxf(x, dpp<XOR2>(x)); x = fmaxf(x, dpp<XOR7>(x)); x = fmaxf(x, dpp<XOR8>(x)); return x;
}
__device__ __forceinline__ float tr_reduce16(float (&x)[16], int c) {
#define TR_STEP(HALF, CTRL, BIT) _Pragma("unroll") for (int hh = 0; hh < (HALF); ++hh) { \
    const float lo_ = x[hh], hi_ = x[hh + (HALF)]; const float t1_ = lo_ + dpp<CTRL>(lo_), t2_ = hi_ + dpp<CTRL>(hi_); \
    x[hh] = (c & (BIT)) ? t2_ : t1_; }
  TR_STEP(8, XOR8, 8) TR_STEP(4, XOR7, 4) TR_STEP(2, XOR2, 2) TR_STEP(1, XOR1, 1)
#undef TR_STEP
  return x[0];
}
__device__ __forceinline__ void put_word(gu32* g, float v) { __hip_atomic_store(g, __float_as_uint(v) | 1u, RLX_AGENT); }
__device__ __forceinline__ void put_word2(gu32* g, float v0, float v1) {
  __hip_atomic_store((gu64*)g, (u64)(__float_as_uint(v0) | 1u) | ((u64)(__float_as_uint(v1) | 1u) << 32), RLX_AGENT);
}
__device__ __forceinline__ u32x4 get_word4(const gu32* g) {
  u32x4 v;
  asm volatile("global_load_dwordx4 %0, %1, off sc1\n\ts_waitcnt vmcnt(0)" : "=&v"(v) : "v"(g) : "memory");
  return v;
}
__device__ __forceinline__ u64 epoch_mix(u64 e) { return e * 6364136223846793005ull + 1442695040888963407ull; }

#define STAMP(k) do {} while (0)
constexpr int L_QB = 0;
constexpr int L_REC = 128;
constexpr int L_OB = L_REC + 1024;
constexpr int L_ML = L_OB + 192;
constexpr int L_F = L_ML + 16;
constexpr int L_G = L_F + 8;
constexpr int L_CNT = L_G + 2;
constexpr int L_UP = L_CNT + 6;
constexpr int L_WT = L_UP + 1024;
constexpr int WT_STRIDE = 44;
constexpr int L_END = L_WT + 192 * WT_STRIDE;
typedef short s16x4 __attribute__((ext_vector_type(4)));
using bf16x8 = __attribute__((ext_vector_type(8))) __bf16;
#define RNG 24
#define WIN 4
#define DMIN (WIN + 2)
constexpr int niss(int i) { const int a = i + 1 + DMIN, b = RNG + (i + 1) / 3; const int m = a > b ? a : b; return m > 64 ? 64 : m; }
#define LAS __attribute__((address_space(3)))
#define USE_NT 3
template <bool NT> __device__ __forceinline__ u32x4 ld16(const u16* p) {
  return NT ? __builtin_nontemporal_load(reinterpret_cast<const u32x4*>(p)) : *reinterpret_cast<const u32x4*>(p);
}
__device__ __forceinline__ u32x4 ldnt(const u16* p) { return ld16<(USE_NT & 4) != 0>(p); }
template <int N> __device__ __forceinline__ void wait_vm() { asm volatile("s_waitcnt vmcnt(%0)" :: "i"(N) : "memory"); }
__device__ __forceinline__ void lds_wait(float* lds, int idx, u32 want) {
  for (u32 spins = 0; spins < (1u << 22); ++spins) {
    if (__builtin_amdgcn_readfirstlane(__hip_atomic_load((LAS u32*)(lds + idx), __ATOMIC_ACQUIRE, __HIP_MEMORY_SCOPE_WORKGROUP)) >= want) break;
    __builtin_amdgcn_s_sleep(1);
  }
}
__device__ __forceinline__ void lds_arrive(float* lds, int idx, int lane) {
  if (lane == 0) __hip_atomic_fetch_add((LAS u32*)(lds + idx), 1u, __ATOMIC_RELEASE, __HIP_MEMORY_SCOPE_WORKGROUP);
}
__device__ __forceinline__ u32 all4(u32x4 v) { return v[0] & v[1] & v[2] & v[3]; }

__global__ void __launch_bounds__(576, 3)
fused_kernel(const u16* __restrict__ x, const u16* __restrict__ kc, const u16* __restrict__ vc,
             const u16* __restrict__ wq, const u16* __restrict__ wo, const u16* __restrict__ w1,
             const u16* __restrict__ w2, const u16* __restrict__ gw, u16* __restrict__ out, unsigned char* ws) {
  __shared__ __attribute__((aligned(16))) float lds[L_END];
  const int tid = threadIdx.x, lane = tid & 63;
  const int wave = __builtin_amdgcn_readfirstlane(tid >> 6);
  const int r = lane >> 4, c = lane & 15;
#define HMAP 1
  const int s = HMAP ? (blockIdx.x >> 3) & 7 : blockIdx.x >> 5;
  const int h = HMAP ? (blockIdx.x >> 6) * 8 + ((blockIdx.x + s + (blockIdx.x >> 6)) & 7) : blockIdx.x & 31;
  const int bid = s * 32 + h;
  STAMP(0);
  if (tid < 4) reinterpret_cast<u32*>(lds)[L_CNT + tid] = 0u;
  gu32* boxes = (gu32*)(ws + WS_BOX);
  gu32* mybox = boxes + (size_t)bid * BX_WORDS;
  u64 tag = 0;

  if (wave < 8) {
    const int b = wave >> 2, wv = wave & 3;
    const size_t kvoff = ((size_t)(b * NH + h) * SK + s * (SK / NS) + 4 * wv + r) * DH + 8 * c;
    const u16* kp = kc + kvoff;
    const u16* vp = vc + kvoff;
    u32x4 ring[RNG];
    __syncthreads();
    const int stg = tid < 480 ? tid : 479;
    const int srg = stg / 10, sch = stg - 10 * srg;
    u32x4 wst[4];
#pragma unroll
    for (int j = 0; j < 4; ++j) {
      const int row = min(srg + 48 * j, 181);
      const u16* p = row < DH ? wo + (size_t)(h * DH + row) * DM : w2 + (size_t)(FH * h + row - DH) * DM;
      wst[j] = ldnt(p + CS * s + 8 * sch);
    }
    __builtin_amdgcn_sched_barrier(0);
    u32x4 w1r[2];
    u32 xm[2][2];
    {
      const int col0 = min((FH * h) & ~7, FF - 64) + 8 * (lane & 7);
#pragma unroll
      for (int t = 0; t < 2; ++t) {
        const int rr = (lane >> 3) + 8 * t, row = CS * s + 10 * wave + (rr < 10 ? rr : 9);
        wait_vm<WIN - 1>();
        w1r[t] = ldnt(w1 + (size_t)row * FF + col0);
        xm[t][0] = x[row]; xm[t][1] = x[DM + row];
        __builtin_amdgcn_sched_barrier(0);
      }
    }
    __builtin_amdgcn_sched_barrier(0);
#define ISSUE(N) { wait_vm<WIN - 1>(); \
      if ((N) < 32) ring[(N) % RNG] = ld16<(USE_NT & 1) != 0>(kp + (size_t)(16 * (N)) * DH); else ring[(N) % RNG] = ld16<(USE_NT & 2) != 0>(vp + (size_t)(16 * ((N) - 32)) * DH); \
      __builtin_amdgcn_sched_barrier(0); }
#pragma unroll
    for (int i = 0; i < RNG; ++i) ISSUE(i)
    STAMP(1);
    {
#pragma unroll
      for (int j = 0; j < 4; ++j) {
        const int row = srg + 48 * j;
        *reinterpret_cast<u32x4*>(lds + L_WT + row * WT_STRIDE + 4 * sch) = row < 182 ? wst[j] : u32x4{0u, 0u, 0u, 0u};
      }
      float u[16];
      const float m1 = lane < 16 ? 1.f : 0.f;
#pragma unroll
      for (int bb = 0; bb < 2; ++bb) {
        const float x0 = bflo(xm[0][bb]), x1 = bflo(xm[1][bb]) * m1;
#pragma unroll
        for (int k = 0; k < 4; ++k) {
          u[bb * 8 + 2 * k] = fmaf(x0, bflo(w1r[0][k]), x1 * bflo(w1r[1][k]));
          u[bb * 8 + 2 * k + 1] = fmaf(x0, bfhi(w1r[0][k]), x1 * bfhi(w1r[1][k]));
        }
      }
      float v8[8], v4[4];
#pragma unroll
      for (int i = 0; i < 16; ++i) u[i] += dpp<XOR8>(u[i]);
#pragma unroll
      for (int i = 0; i < 8; ++i) v8[i] = sw16_add(u[i], u[i + 8]);
#pragma unroll
      for (int i = 0; i < 4; ++i) v4[i] = sw32_add(v8[i], v8[i + 4]);
      if (c < 8) {
        float* dst = lds + L_UP + wave * 128 + 2 * (8 * c + 4 * (r >> 1)) + (r & 1);
#pragma unroll
        for (int i = 0; i < 4; ++i) dst[2 * i] = v4[i];
      }
      lds_arrive(lds, L_CNT + 3, lane);
    }
    lds_wait(lds, L_CNT, 1);
    const u32x4 qp = *reinterpret_cast<const u32x4*>(lds + L_QB + b * 64 + 4 * c);
    STAMP(2);
    float sc[2][16];
#pragma unroll
    for (int i = 0; i < 32; ++i) {
      float d = 0.f;
#pragma unroll
      for (int k = 0; k < 4; ++k) d = dot2(ring[i % RNG][k], qp[k], d);
      asm volatile("" : "+v"(d));
      sc[i >> 4][i & 15] = d;
#pragma unroll
      for (int n = niss(i - 1); n < niss(i); ++n) ISSUE(n)
    }
    STAMP(3);
    float p0, p1;
    {
      float s0 = tr_reduce16(sc[0], c), s1 = tr_reduce16(sc[1], c);
      s0 = bf16r(bf16r(s0) * 0.08838834764831845f);
      s1 = bf16r(bf16r(s1) * 0.08838834764831845f);
      const float mw = x32_max(x16_max(row_max(fmaxf(s0, s1))));
      p0 = __builtin_amdgcn_exp2f((s0 - mw) * 1.4426950408889634f);
      p1 = __builtin_amdgcn_exp2f((s1 - mw) * 1.4426950408889634f);
      const float lw = x32_sum(x16_sum(row_sum(p0 + p1)));
      if (lane == 0) *reinterpret_cast<f32x2*>(lds + L_ML + 2 * wave) = f32x2{mw, lw};
      lds_arrive(lds, L_CNT + 1, lane);
    }
    float acc[8];
#pragma unroll
    for (int j = 0; j < 8; ++j) acc[j] = 0.f;
#define PV_STEP(I) { const float pi = dpp<0x150 + ((I) & 15)>((I) < 16 ? p0 : p1); const u32x4 v = ring[(32 + (I)) % RNG]; \
      _Pragma("unroll") for (int k = 0; k < 4; ++k) { acc[2 * k] = fmaf(pi, bflo(v[k]), acc[2 * k]); acc[2 * k + 1] = fmaf(pi, bfhi(v[k]), acc[2 * k + 1]); } \
      asm volatile("" : "+v"(acc[0]), "+v"(acc[1]), "+v"(acc[2]), "+v"(acc[3]), "+v"(acc[4]), "+v"(acc[5]), "+v"(acc[6]), "+v"(acc[7])); \
      _Pragma("unroll") for (int n = niss(31 + (I)); n < niss(32 + (I)); ++n) ISSUE(n) }
    PV_STEP(0) PV_STEP(1) PV_STEP(2) PV_STEP(3) PV_STEP(4) PV_STEP(5) PV_STEP(6) PV_STEP(7)
    PV_STEP(8) PV_STEP(9) PV_STEP(10) PV_STEP(11) PV_STEP(12) PV_STEP(13) PV_STEP(14) PV_STEP(15)
    PV_STEP(16) PV_STEP(17) PV_STEP(18) PV_STEP(19) PV_STEP(20) PV_STEP(21) PV_STEP(22) PV_STEP(23)
    PV_STEP(24) PV_STEP(25) PV_STEP(26) PV_STEP(27) PV_STEP(28) PV_STEP(29) PV_STEP(30) PV_STEP(31)
#undef PV_STEP
#undef ISSUE
    {
      float u[4], t[2];
#pragma unroll
      for (int i = 0; i < 4; ++i) u[i] = sw16_add(acc[i], acc[i + 4]);
#pragma unroll
      for (int i = 0; i < 2; ++i) t[i] = sw32_add(u[i], u[i + 2]);
      lds_wait(lds, L_CNT + 2, 1);
      const float f = lds[L_F + wave];
      *reinterpret_cast<f32x2*>(lds + L_REC + wave * 128 + 8 * c + 4 * (r & 1) + 2 * (r >> 1)) = f32x2{t[0] * f, t[1] * f};
    }
  } else {
#pragma unroll
    for (int i = 0; i < 7; ++i) __hip_atomic_store((gu64*)mybox + min(lane + 64 * i, BX_WORDS / 2 - 1), 0ull, RLX_AGENT);
    asm volatile("" ::: "memory");
    tag = __hip_atomic_load((gu64*)(ws + WS_E), RLX_AGENT);
    u32 xq0, xq1;
    u64 fl0;
    {
      const u16* p = x + CS * s + 8 * (c < 10 ? c : 9) + 2 * r;
      xq0 = *reinterpret_cast<const u32*>(p); xq1 = *reinterpret_cast<const u32*>(p + DM);
    }
    {
      u32x4 wqr[20];
      const u16* p = wq + (size_t)(CS * s + 2 * r) * (NH * DH) + h * DH + 8 * c;
#pragma unroll
      for (int j = 0; j < 10; ++j) {
        wqr[2 * j] = ldnt(p + (size_t)(8 * j) * (NH * DH));
        wqr[2 * j + 1] = ldnt(p + (size_t)(8 * j + 1) * (NH * DH));
      }
      __builtin_amdgcn_sched_barrier(0);
      __builtin_amdgcn_s_barrier();
      __builtin_amdgcn_sched_barrier(0);
      wait_vm<22>();
      asm volatile("" : "+v"(tag));
      tag = epoch_mix(tag);
      __hip_atomic_store((gu64*)(ws + WS_FLAG + (size_t)bid * 128), tag, RLX_AGENT);
      __builtin_amdgcn_sched_barrier(0);
      fl0 = __hip_atomic_load((gu64*)(ws + WS_FLAG + (size_t)((lane & 7) * 32 + h) * 128), RLX_AGENT);
      __builtin_amdgcn_sched_barrier(0);
      float qa[16];
#pragma unroll
      for (int j = 0; j < 16; ++j) qa[j] = 0.f;
#define Q_STEP(J) { const u32 x0 = dppu<0x150 + (J)>(xq0), x1 = dppu<0x150 + (J)>(xq1); \
      _Pragma("unroll") for (int k = 0; k < 4; ++k) { \
        const u32 lo = __builtin_amdgcn_perm(wqr[2 * (J) + 1][k], wqr[2 * (J)][k], 0x05040100u); \
        const u32 hi = __builtin_amdgcn_perm(wqr[2 * (J) + 1][k], wqr[2 * (J)][k], 0x07060302u); \
        qa[2 * k] = dot2(lo, x0, qa[2 * k]); qa[2 * k + 1] = dot2(hi, x0, qa[2 * k + 1]); \
        qa[8 + 2 * k] = dot2(lo, x1, qa[8 + 2 * k]); qa[8 + 2 * k + 1] = dot2(hi, x1, qa[8 + 2 * k + 1]); } }
      Q_STEP(0) Q_STEP(1) Q_STEP(2) Q_STEP(3) Q_STEP(4) Q_STEP(5) Q_STEP(6) Q_STEP(7) Q_STEP(8) Q_STEP(9)
#undef Q_STEP
      float u[8], t[4];
#pragma unroll
      for (int i = 0; i < 8; ++i) u[i] = sw16_add(qa[i], qa[i + 8]);
#pragma unroll
      for (int i = 0; i < 4; ++i) t[i] = sw32_add(u[i], u[i + 4]);
      gu32* dst = mybox + BX_Q + (r & 1) * 128 + 8 * c + 4 * (r >> 1);
      put_word2(dst, t[0], t[1]); put_word2(dst + 2, t[2], t[3]);
    }
    STAMP(1);
    gu64* fptr = (gu64*)(ws + WS_FLAG + (size_t)(lane < 8 ? lane * 32 + h : s * 32 + ((lane - 8) & 31)) * 128);
    {
      const gu32* qsrc = boxes + (size_t)h * BX_WORDS + BX_Q + (r & 1) * 128 + 8 * c + 4 * (r >> 1);
      constexpr size_t GS = (size_t)32 * BX_WORDS;
      u32x4 g[8];
      bool flags_ok = false;
      for (u32 spins = 0; spins < SPIN_CAP; ++spins) {
        u64 fl;
        asm volatile("global_load_dwordx2 %8, %17, off sc1\n\t"
                     "global_load_dwordx4 %0, %9, off sc1\n\tglobal_load_dwordx4 %1, %10, off sc1\n\t"
                     "global_load_dwordx4 %2, %11, off sc1\n\tglobal_load_dwordx4 %3, %12, off sc1\n\t"
                     "global_load_dwordx4 %4, %13, off sc1\n\tglobal_load_dwordx4 %5, %14, off sc1\n\t"
                     "global_load_dwordx4 %6, %15, off sc1\n\tglobal_load_dwordx4 %7, %16, off sc1\n\ts_waitcnt vmcnt(0)"
                     : "=&v"(g[0]), "=&v"(g[1]), "=&v"(g[2]), "=&v"(g[3]), "=&v"(g[4]), "=&v"(g[5]), "=&v"(g[6]), "=&v"(g[7]), "=&v"(fl)
                     : "v"(qsrc), "v"(qsrc + GS), "v"(qsrc + 2 * GS), "v"(qsrc + 3 * GS), "v"(qsrc + 4 * GS), "v"(qsrc + 5 * GS), "v"(qsrc + 6 * GS), "v"(qsrc + 7 * GS), "v"(fptr)
                     : "memory");
        flags_ok = flags_ok || __all(lane >= 8 || fl0 == tag);
        const u32 ok = all4(g[0]) & all4(g[1]) & all4(g[2]) & all4(g[3]) & all4(g[4]) & all4(g[5]) & all4(g[6]) & all4(g[7]);
        if (flags_ok && __all(ok & 1u)) break;
        fl0 = fl;
      }
      STAMP(2);
      f32x4 q = {0.f, 0.f, 0.f, 0.f};
#pragma unroll
      for (int sp = 0; sp < 8; ++sp) q += __builtin_bit_cast(f32x4, g[sp]);
      u32x2 pk = {(u32)bf16bits(q[0]) | ((u32)bf16bits(q[1]) << 16), (u32)bf16bits(q[2]) | ((u32)bf16bits(q[3]) << 16)};
      *reinterpret_cast<u32x2*>(lds + L_QB + (r & 1) * 64 + 4 * c + 2 * (r >> 1)) = pk;
      lds_arrive(lds, L_CNT, lane);
    }
    STAMP(3);
    lds_wait(lds, L_CNT + 3, 8);
    {
      const int cw = FH * h - min((FH * h) & ~7, FF - 64) + (lane < FH ? lane : 0);
      f32x2 u = {0.f, 0.f};
#pragma unroll
      for (int w = 0; w < 8; ++w) u += *reinterpret_cast<const f32x2*>(lds + L_UP + w * 128 + 2 * cw);
      if (lane < FH) put_word2(mybox + BX_U + 2 * lane, u[0], u[1]);
    }
    {
      float g0 = 0.f, g1 = 0.f;
#pragma unroll
      for (int t = 0; t < 5; ++t) {
        const u32 w = *reinterpret_cast<const u32*>(gw + 2 * lane + 128 * t);
        const u32 xa = *reinterpret_cast<const u32*>(x + 2 * lane + 128 * t), xb = *reinterpret_cast<const u32*>(x + DM + 2 * lane + 128 * t);
        g0 += bf16r(bflo(xa) * bflo(w)); g1 += bf16r(bflo(xb) * bflo(w));
        g0 += bf16r(bfhi(xa) * bfhi(w)); g1 += bf16r(bfhi(xb) * bfhi(w));
      }
      g0 = x32_sum(x16_sum(row_sum(g0))); g1 = x32_sum(x16_sum(row_sum(g1)));
      g0 = bf16r(g0); g1 = bf16r(g1);
      g0 = bf16r(1.f / (1.f + __expf(-g0))); g1 = bf16r(1.f / (1.f + __expf(-g1)));
      if (lane == 0) { lds[L_G] = g0; lds[L_G + 1] = g1; }
      STAMP(4);
      lds_wait(lds, L_CNT + 1, 8);
      const int wl = lane & 7;
      const f32x2 ml = *reinterpret_cast<const f32x2*>(lds + L_ML + 2 * wl);
      float M = fmaxf(ml[0], dpp<XOR1>(ml[0])); M = fmaxf(M, dpp<XOR2>(M));
      float L = ml[1] * __builtin_amdgcn_exp2f((ml[0] - M) * 1.4426950408889634f);
      L += dpp<XOR1>(L); L += dpp<XOR2>(L);
      if (lane == 0 || lane == 4) put_word2(mybox + BX_ML + (lane >> 1), M, L);
      const gu32* msrc = boxes + (size_t)(wl * 32 + h) * BX_WORDS + BX_ML;
      u32x4 gm;
      for (u32 spins = 0; spins < SPIN_CAP; ++spins) {
        gm = get_word4(msrc);
        if (__all(all4(gm) & 1u)) break;
        __builtin_amdgcn_s_sleep(2);
      }
      float fsc[2];
#pragma unroll
      for (int bb = 0; bb < 2; ++bb) {
        const float Ms = __uint_as_float(gm[2 * bb]), Ls = __uint_as_float(gm[2 * bb + 1]);
        float Mg = fmaxf(Ms, dpp<XOR1>(Ms)); Mg = fmaxf(Mg, dpp<XOR2>(Mg)); Mg = fmaxf(Mg, dpp<XOR7>(Mg));
        float Lg = Ls * __builtin_amdgcn_exp2f((Ms - Mg) * 1.4426950408889634f);
        Lg += dpp<XOR1>(Lg); Lg += dpp<XOR2>(Lg); Lg += dpp<XOR7>(Lg);
        fsc[bb] = __builtin_amdgcn_exp2f((ml[0] - Mg) * 1.4426950408889634f) / Lg;
      }
      if (lane < 8) lds[L_F + lane] = lane < 4 ? fsc[0] : fsc[1];
      lds_arrive(lds, L_CNT + 2, lane);
      u64 ug[8];
      for (u32 spins = 0; spins < SPIN_CAP; ++spins) {
        u64 all = ~0ull;
#pragma unroll
        for (int sp = 0; sp < 8; ++sp) {
          ug[sp] = __hip_atomic_load((gu64*)(boxes + (size_t)(sp * 32 + h) * BX_WORDS + BX_U) + (lane < FH ? lane : 0), RLX_AGENT);
          all &= ug[sp];
        }
        if (__all((all & 0x100000001ull) == 0x100000001ull)) break;
        __builtin_amdgcn_s_sleep(2);
      }
      float u0 = 0.f, u1 = 0.f;
#pragma unroll
      for (int sp = 0; sp < 8; ++sp) { u0 += __uint_as_float((u32)ug[sp]); u1 += __uint_as_float((u32)(ug[sp] >> 32)); }
      u0 = bf16r(u0); u1 = bf16r(u1);
      u16* ob = reinterpret_cast<u16*>(lds + L_OB);
      ob[DH + lane] = lane < FH ? bf16bits(u0 / (1.f + __expf(-u0))) : (u16)0;
      ob[192 + DH + lane] = lane < FH ? bf16bits(u1 / (1.f + __expf(-u1))) : (u16)0;
    }
    for (u32 spins = 0; spins < SPIN_CAP; ++spins) {
      const u64 fl = __hip_atomic_load(fptr, RLX_AGENT);
      if (__all(lane >= 40 || fl == tag)) break;
      __builtin_amdgcn_s_sleep(2);
    }
  }
  if (wave < 8) STAMP(4);
  __syncthreads();
  STAMP(5);

  if (wave < 4) {
    const float* rec = lds + L_REC + (tid >> 7) * 512 + (tid & 127);
    put_word(mybox + BX_A + tid, (rec[0] + rec[128]) + (rec[256] + rec[384]));
  }
  u32x4 wg[6];
  {
    float* wsrc = lds + L_WT + (8 * r + (c >> 2)) * WT_STRIDE + 8 * (wave < 5 ? wave : 4) + 2 * (c & 3);
#pragma unroll
    for (int ks = 0; ks < 6; ++ks) {
      const s16x4 lo = __builtin_amdgcn_ds_read_tr16_b64_v4i16((LAS s16x4*)(wsrc + (32 * ks) * WT_STRIDE));
      const s16x4 hi = __builtin_amdgcn_ds_read_tr16_b64_v4i16((LAS s16x4*)(wsrc + (32 * ks + 4) * WT_STRIDE));
      const u32x2 l2 = __builtin_bit_cast(u32x2, lo), h2 = __builtin_bit_cast(u32x2, hi);
      wg[ks] = u32x4{l2[0], l2[1], h2[0], h2[1]};
    }
  }
  if (wave < 8) {
    const gu32* src = boxes + (size_t)((lane >> 3) * 32 + h) * BX_WORDS + BX_A + 32 * wave + 4 * (lane & 7);
    u32x4 ga;
    for (u32 spins = 0; spins < SPIN_CAP; ++spins) {
      ga = get_word4(src);
      if (__all(all4(ga) & 1u)) break;
    }
    float o[4];
#pragma unroll
    for (int k = 0; k < 4; ++k) { float t = __uint_as_float(ga[k]); t += dpp<XOR8>(t); o[k] = x32_sum(x16_sum(t)); }
    if (lane < 8) {
      u32x2 pk = {(u32)bf16bits(o[0]) | ((u32)bf16bits(o[1]) << 16), (u32)bf16bits(o[2]) | ((u32)bf16bits(o[3]) << 16)};
      *reinterpret_cast<u32x2*>(lds + L_OB + (wave >> 2) * 96 + 16 * (wave & 3) + 2 * lane) = pk;
    }
  }
  __syncthreads();
  STAMP(6);

  if (wave < 5) {
    f32x4 ya = {0.f, 0.f, 0.f, 0.f}, yb = {0.f, 0.f, 0.f, 0.f};
    const float* arow = lds + L_OB + (c & 1) * 96 + 4 * r;
#pragma unroll
    for (int ks = 0; ks < 6; ++ks) {
      u32x4 af = *reinterpret_cast<const u32x4*>(arow + 16 * ks);
      if (c >= 2) af = u32x4{0u, 0u, 0u, 0u};
      if (ks < 4) ya = __builtin_amdgcn_mfma_f32_16x16x32_bf16(__builtin_bit_cast(bf16x8, af), __builtin_bit_cast(bf16x8, wg[ks]), ya, 0, 0, 0);
      else yb = __builtin_amdgcn_mfma_f32_16x16x32_bf16(__builtin_bit_cast(bf16x8, af), __builtin_bit_cast(bf16x8, wg[ks]), yb, 0, 0, 0);
    }
    if (lane < 16) {
      const float g0 = lds[L_G], g1 = lds[L_G + 1];
      put_word(mybox + BX_B + 16 * wave + lane, fmaf(g0, ya[0], bf16r(1.f - g0) * yb[0]));
      put_word(mybox + BX_B + CS + 16 * wave + lane, fmaf(g1, ya[1], bf16r(1.f - g1) * yb[1]));
    }
  }
  STAMP(7);
  if (tid < 160) {
    const int hp = tid & 31, idx = 5 * h + (tid >> 5);
    gu32* g = boxes + (size_t)(s * 32 + hp) * BX_WORDS + BX_B + idx;
    u32 gv = 0;
    for (u32 spins = 0; spins < SPIN_CAP; ++spins) {
      gv = __hip_atomic_load(g, RLX_AGENT);
      if (__all(gv & 1u)) break;
      __builtin_amdgcn_s_sleep(BSLEEP);
    }
    const float t = x16_sum(row_sum(__uint_as_float(gv)));
    if (hp == 0) { const int bb = idx / CS, col = idx - bb * CS; out[bb * DM + CS * s + col] = bf16bits(t); }
  }
  if (bid == 0) {
    __syncthreads();
    if (tid == 512) __hip_atomic_store((gu64*)(ws + WS_E), tag, RLX_AGENT);
  }
}

extern "C" void kernel_launch(void* const* d_in, const int* in_sizes, int n_in,
                              void* d_out, int out_size, void* d_ws, size_t ws_size,
                              hipStream_t stream) {
  const u16* x = (const u16*)d_in[0];
  const u16* kc = (const u16*)d_in[1];
  const u16* vc = (const u16*)d_in[2];
  const u16* wq = (const u16*)d_in[3];
  const u16* wo = (const u16*)d_in[4];
  const u16* w1 = (const u16*)d_in[6];
  const u16* w2 = (const u16*)d_in[7];
  const u16* gw = (const u16*)d_in[8];
  fused_kernel<<<256, 576, 0, stream>>>(x, kc, vc, wq, wo, w1, w2, gw, (u16*)d_out, (unsigned char*)d_ws);
}
```

```cpp
#include <hip/hip_runtime.h>
#include <hip/hip_bf16.h>
#include <stdint.h>

typedef unsigned int u32;
typedef unsigned short u16;
typedef unsigned long long u64;
using u32x2 = __attribute__((ext_vector_type(2))) unsigned;
using u32x4 = __attribute__((ext_vector_type(4))) unsigned;
using f32x2 = __attribute__((ext_vector_type(2))) float;
using f32x4 = __attribute__((ext_vector_type(4))) float;
using bf16x2 = __attribute__((ext_vector_type(2))) __bf16;
#define GAS __attribute__((address_space(1)))
typedef GAS u64 gu64;
typedef GAS u32 gu32;
#define RLX_AGENT __ATOMIC_RELAXED, __HIP_MEMORY_SCOPE_AGENT

constexpr int DM = 640, NH = 32, SK = 4096, DH = 128, FF = 1728;
constexpr int NS = 8;
constexpr int FH = 64;
constexpr int NFG = FF / FH;
constexpr int CS = DM / NS;
constexpr size_t WS_E = 0;
constexpr size_t WS_FLAG = 4096;
constexpr size_t WS_BOX = 65536;
constexpr int BX_A = 0;
constexpr int BX_ML = 256;
constexpr int BX_B = 288;
constexpr int BX_CLR = 448;
constexpr int BX_Q = 448;
constexpr int BX_U = 704;
constexpr int BX_WORDS = 832;
constexpr size_t BOX_BYTES = BX_WORDS * 4;
constexpr u32 SPIN_CAP = 1u << 18;
#define QSLEEP 2
#define MSLEEP 16
#define ASLEEP 0
#define BSLEEP 1
#define STAMPW 0

__device__ __forceinline__ float dot2(u32 k, u32 q, float acc) {
  return __builtin_amdgcn_fdot2_f32_bf16(__builtin_bit_cast(bf16x2, k), __builtin_bit_cast(bf16x2, q), acc, false);
}
__device__ __forceinline__ float bflo(u32 v) { return __builtin_bit_cast(float, v << 16); }
__device__ __forceinline__ float bfhi(u32 v) { return __builtin_bit_cast(float, v & 0xffff0000u); }
__device__ __forceinline__ float bf16r(float x) { return (float)(__bf16)x; }
__device__ __forceinline__ u16 bf16bits(float x) { return __builtin_bit_cast(u16, (__bf16)x); }
template <int CTRL> __device__ __forceinline__ float dpp(float x) {
  return __builtin_bit_cast(float, __builtin_amdgcn_mov_dpp(__builtin_bit_cast(int, x), CTRL, 0xf, 0xf, true));
}
template <int CTRL> __device__ __forceinline__ u32 dppu(u32 x) {
  return (u32)__builtin_amdgcn_mov_dpp((int)x, CTRL, 0xf, 0xf, true);
}
constexpr int XOR1 = 0xB1, XOR2 = 0x4E, XOR7 = 0x141, XOR8 = 0x128;
__device__ __forceinline__ float sw16_add(float a, float b) {
  auto s = __builtin_amdgcn_permlane16_swap(__float_as_uint(a), __float_as_uint(b), false, false);
  return __uint_as_float(s[0]) + __uint_as_float(s[1]);
}
__device__ __forceinline__ float sw32_add(float a, float b) {
  auto t = __builtin_amdgcn_permlane32_swap(__float_as_uint(a), __float_as_uint(b), false, false);
  return __uint_as_float(t[0]) + __uint_as_float(t[1]);
}
__device__ __forceinline__ float x16_sum(float x) { return sw16_add(x, x); }
__device__ __forceinline__ float x32_sum(float x) { return sw32_add(x, x); }
__device__ __forceinline__ float x16_max(float x) {
  auto s = __builtin_amdgcn_permlane16_swap(__float_as_uint(x), __float_as_uint(x), false, false);
  return fmaxf(__uint_as_float(s[0]), __uint_as_float(s[1]));
}
__device__ __forceinline__ float x32_max(float x) {
  auto t = __builtin_amdgcn_permlane32_swap(__float_as_uint(x), __float_as_uint(x), false, false);
  return fmaxf(__uint_as_float(t[0]), __uint_as_float(t[1]));
}
__device__ __forceinline__ float row_sum(float x) {
  x += dpp<XOR1>(x); x += dpp<XOR2>(x); x += dpp<XOR7>(x); x += dpp<XOR8>(x); return x;
}
__device__ __forceinline__ float row_max(float x) {
  x = fmaxf(x, dpp<XOR1>(x)); x = fmaxf(x, dpp<XOR2>(x)); x = fmaxf(x, dpp<XOR7>(x)); x = fmaxf(x, dpp<XOR8>(x)); return x;
}
__device__ __forceinline__ float tr_reduce16(float (&x)[16], int c) {
#define TR_STEP(HALF, CTRL, BIT) _Pragma("unroll") for (int hh = 0; hh < (HALF); ++hh) { \
    const float lo_ = x[hh], hi_ = x[hh + (HALF)]; const float t1_ = lo_ + dpp<CTRL>(lo_), t2_ = hi_ + dpp<CTRL>(hi_); \
    x[hh] = (c & (BIT)) ? t2_ : t1_; }
  TR_STEP(8, XOR8, 8) TR_STEP(4, XOR7, 4) TR_STEP(2, XOR2, 2) TR_STEP(1, XOR1, 1)
#undef TR_STEP
  return x[0];
}
__device__ __forceinline__ void put_word(gu32* g, float v) { __hip_atomic_store(g, __float_as_uint(v) | 1u, RLX_AGENT); }
__device__ __forceinline__ void put_word2(gu32* g, float v0, float v1) {
  __hip_atomic_store((gu64*)g, (u64)(__float_as_uint(v0) | 1u) | ((u64)(__float_as_uint(v1) | 1u) << 32), RLX_AGENT);
}
__device__ __forceinline__ u32x4 get_word4(const gu32* g) {
  u32x4 v;
  asm volatile("global_load_dwordx4 %0, %1, off sc1\n\ts_waitcnt vmcnt(0)" : "=&v"(v) : "v"(g) : "memory");
  return v;
}
__device__ __forceinline__ void put_raw2(gu32* g, float v0, float v1) {
  __hip_atomic_store((gu64*)g, (u64)__float_as_uint(v0) | ((u64)__float_as_uint(v1) << 32), RLX_AGENT);
}
__device__ __forceinline__ u64 epoch_mix(u64 e) { return e * 6364136223846793005ull + 1442695040888963407ull; }

#define STAMP(k) do {} while (0)
constexpr int L_QB = 0;
constexpr int L_REC = 128;
constexpr int L_OB = L_REC + 8 * 132;
constexpr int L_ML = L_OB + 192;
constexpr int L_F = L_ML + 16;
constexpr int L_G = L_F + 16;
constexpr int L_CNT = L_G + 2;
constexpr int L_UP = L_CNT + 6;
constexpr int L_WT = L_UP + 1024;
constexpr int WT_STRIDE = 44;
constexpr int L_END = L_WT + 192 * WT_STRIDE;
typedef short s16x4 __attribute__((ext_vector_type(4)));
using bf16x8 = __attribute__((ext_vector_type(8))) __bf16;
#define RNG 24
#define WIN 4
#define AUXAT 6
#define WIN2 WIN
#define DMIN (WIN2 + 2)
#define BUMP 2
constexpr int niss(int i) { const int a = i + 1 + DMIN + ((i >= 31 && i < 31 + BUMP) ? 31 + BUMP - i : 0), b = RNG + (i + 1) / 3; const int m = a > b ? a : b; return m > 64 ? 64 : m; }
#define LAS __attribute__((address_space(3)))
#define USE_NT 7
template <bool NT> __device__ __forceinline__ u32x4 ld16(const u16* p) {
  return NT ? __builtin_nontemporal_load(reinterpret_cast<const u32x4*>(p)) : *reinterpret_cast<const u32x4*>(p);
}
__device__ __forceinline__ u32x4 ldnt(const u16* p) { return ld16<(USE_NT & 4) != 0>(p); }
template <int N> __device__ __forceinline__ void wait_vm() { asm volatile("s_waitcnt vmcnt(%0)" :: "i"(N) : "memory"); }
__device__ __forceinline__ void lds_wait(float* lds, int idx, u32 want) {
  for (u32 spins = 0; spins < (1u << 22); ++spins) {
    if (__builtin_amdgcn_readfirstlane(__hip_atomic_load((LAS u32*)(lds + idx), __ATOMIC_ACQUIRE, __HIP_MEMORY_SCOPE_WORKGROUP)) >= want) break;
    __builtin_amdgcn_s_sleep(1);
  }
}
__device__ __forceinline__ void lds_arrive(float* lds, int idx, int lane) {
  if (lane == 0) __hip_atomic_fetch_add((LAS u32*)(lds + idx), 1u, __ATOMIC_RELEASE, __HIP_MEMORY_SCOPE_WORKGROUP);
}
__device__ __forceinline__ u32 all4(u32x4 v) { return v[0] & v[1] & v[2] & v[3]; }

__global__ void __launch_bounds__(576, 3)
fused_kernel(const u16* __restrict__ x, const u16* __restrict__ kc, const u16* __restrict__ vc,
             const u16* __restrict__ wq, const u16* __restrict__ wo, const u16* __restrict__ w1,
             const u16* __restrict__ w2, const u16* __restrict__ gw, u16* __restrict__ out, unsigned char* ws) {
  __shared__ __attribute__((aligned(16))) float lds[L_END];
  const int tid = threadIdx.x, lane = tid & 63;
  const int wave = __builtin_amdgcn_readfirstlane(tid >> 6);
  const int r = lane >> 4, c = lane & 15;
#define HMAP 2
  const int bk = blockIdx.x >> 5;
  const int s = HMAP == 2 ? 4 * (bk & 1) + ((blockIdx.x >> 3) & 3) : (blockIdx.x >> 3) & 7;
  const int h = HMAP == 2 ? 8 * (bk >> 1) + ((blockIdx.x + 3 * bk) & 7) : (blockIdx.x >> 6) * 8 + ((blockIdx.x + s + (blockIdx.x >> 6)) & 7);
  const bool has_f = h < NFG;
  const int bid = s * 32 + h;
  STAMP(0);
  if (tid < 6) reinterpret_cast<u32*>(lds)[L_CNT + tid] = 0u;
  gu32* boxes = (gu32*)(ws + WS_BOX);
  gu32* mybox = boxes + (size_t)bid * BX_WORDS;
  u64 tag = 0;

  if (wave < 8) {
    const int b = wave >> 2, wv = wave & 3;
    const size_t kvoff = ((size_t)(b * NH + h) * SK + s * (SK / NS) + 4 * wv + r) * DH + 8 * c;
    const u16* kp = kc + kvoff;
    const u16* vp = vc + kvoff;
    u32x4 ring[RNG];
    asm volatile("s_waitcnt lgkmcnt(0)" ::: "memory");
    __builtin_amdgcn_s_barrier();
    __builtin_amdgcn_sched_barrier(0);
    u32x4 w1r[2];
    u32 xm[2][2];
    {
      const int col0 = (has_f ? FH * h : 0) + 8 * (lane & 7);
#pragma unroll
      for (int t = 0; t < 2; ++t) {
        const int rr = (lane >> 3) + 8 * t, row = CS * s + 10 * wave + (rr < 10 ? rr : 9);
        xm[t][0] = x[row]; xm[t][1] = x[DM + row];
      }
      __builtin_amdgcn_sched_barrier(0);
#pragma unroll
      for (int t = 0; t < 2; ++t) {
        const int rr = (lane >> 3) + 8 * t, row = CS * s + 10 * wave + (rr < 10 ? rr : 9);
        w1r[t] = ldnt(w1 + (size_t)row * FF + col0);
      }
    }
    const int stg = tid < 480 ? tid : 479;
    const int srg = stg / 10, sch = stg - 10 * srg;
    u32x4 wst[4];
#pragma unroll
    for (int j = 0; j < 4; ++j) {
      const int row = srg + 48 * j;
      const u16* p = row < DH ? wo + (size_t)(h * DH + row) * DM : (has_f ? w2 + (size_t)(FH * h + row - DH) * DM : wo + (size_t)(h * DH + srg) * DM);
      wst[j] = ldnt(p + CS * s + 8 * sch);
    }
    __builtin_amdgcn_sched_barrier(0);
#define ISSUE(N) { if ((N) < RNG) wait_vm<WIN - 1>(); else if (!((N) > 32 + DMIN - 1 && (N) < 32 + DMIN + BUMP)) wait_vm<WIN2 - 1>(); \
      if ((N) < 32) ring[(N) % RNG] = ld16<(USE_NT & 1) != 0>(kp + (size_t)(16 * (N)) * DH); else ring[(N) % RNG] = ld16<(USE_NT & 2) != 0>(vp + (size_t)(16 * ((N) - 32)) * DH); \
      __builtin_amdgcn_sched_barrier(0); }
#pragma unroll
    for (int i = 0; i < AUXAT; ++i) ISSUE(i)
    {
#pragma unroll
      for (int j = 0; j < 4; ++j) {
        const int row = srg + 48 * j;
        *reinterpret_cast<u32x4*>(lds + L_WT + row * WT_STRIDE + 4 * sch) = (row < DH || has_f) ? wst[j] : u32x4{0u, 0u, 0u, 0u};
      }
      float u[16];
      const float m1 = lane < 16 ? 1.f : 0.f;
#pragma unroll
      for (int bb = 0; bb < 2; ++bb) {
        const float x0 = bflo(xm[0][bb]), x1 = bflo(xm[1][bb]) * m1;
#pragma unroll
        for (int k = 0; k < 4; ++k) {
          u[bb * 8 + 2 * k] = fmaf(x0, bflo(w1r[0][k]), x1 * bflo(w1r[1][k]));
          u[bb * 8 + 2 * k + 1] = fmaf(x0, bfhi(w1r[0][k]), x1 * bfhi(w1r[1][k]));
        }
      }
      float v8[8], v4[4];
#pragma unroll
      for (int i = 0; i < 16; ++i) u[i] += dpp<XOR8>(u[i]);
#pragma unroll
      for (int i = 0; i < 8; ++i) v8[i] = sw16_add(u[i], u[i + 8]);
#pragma unroll
      for (int i = 0; i < 4; ++i) v4[i] = sw32_add(v8[i], v8[i + 4]);
      if (c < 8) {
        float* dst = lds + L_UP + wave * 128 + 2 * (8 * c + 4 * (r >> 1)) + (r & 1);
#pragma unroll
        for (int i = 0; i < 4; ++i) dst[2 * i] = v4[i];
      }
      lds_arrive(lds, L_CNT + 3, lane);
    }
#pragma unroll
    for (int i = AUXAT; i < RNG; ++i) ISSUE(i)
    STAMP(1);
    lds_wait(lds, L_CNT, 1);
    const u32x4 qp = *reinterpret_cast<const u32x4*>(lds + L_QB + b * 64 + 4 * c);
    STAMP(2);
    float sc[2][16];
#pragma unroll
    for (int i = 0; i < 32; ++i) {
      float d = 0.f;
#pragma unroll
      for (int k = 0; k < 4; ++k) d = dot2(ring[i % RNG][k], qp[k], d);
      asm volatile("" : "+v"(d));
      sc[i >> 4][i & 15] = d;
#pragma unroll
      for (int n = niss(i - 1); n < niss(i); ++n) ISSUE(n)
    }
    STAMP(3);
    float p0, p1, mw, lw;
    {
      float s0 = tr_reduce16(sc[0], c), s1 = tr_reduce16(sc[1], c);
      s0 = bf16r(bf16r(s0) * 0.08838834764831845f);
      s1 = bf16r(bf16r(s1) * 0.08838834764831845f);
      mw = x32_max(x16_max(row_max(fmaxf(s0, s1))));
      p0 = __builtin_amdgcn_exp2f((s0 - mw) * 1.4426950408889634f);
      p1 = __builtin_amdgcn_exp2f((s1 - mw) * 1.4426950408889634f);
      lw = x32_sum(x16_sum(row_sum(p0 + p1)));
      if (lane == 0) *reinterpret_cast<f32x2*>(lds + L_ML + 2 * wave) = f32x2{mw, lw};
      lds_arrive(lds, L_CNT + 1, lane);
    }
    float acc[8];
#pragma unroll
    for (int j = 0; j < 8; ++j) acc[j] = 0.f;
#define PV_STEP(I) { const float pi = dpp<0x150 + ((I) & 15)>((I) < 16 ? p0 : p1); const u32x4 v = ring[(32 + (I)) % RNG]; \
      _Pragma("unroll") for (int k = 0; k < 4; ++k) { acc[2 * k] = fmaf(pi, bflo(v[k]), acc[2 * k]); acc[2 * k + 1] = fmaf(pi, bfhi(v[k]), acc[2 * k + 1]); } \
      asm volatile("" : "+v"(acc[0]), "+v"(acc[1]), "+v"(acc[2]), "+v"(acc[3]), "+v"(acc[4]), "+v"(acc[5]), "+v"(acc[6]), "+v"(acc[7])); \
      _Pragma("unroll") for (int n = niss(31 + (I)); n < niss(32 + (I)); ++n) ISSUE(n) }
    PV_STEP(0) PV_STEP(1) PV_STEP(2) PV_STEP(3) PV_STEP(4) PV_STEP(5) PV_STEP(6) PV_STEP(7)
    PV_STEP(8) PV_STEP(9) PV_STEP(10) PV_STEP(11) PV_STEP(12) PV_STEP(13) PV_STEP(14) PV_STEP(15)
    PV_STEP(16) PV_STEP(17) PV_STEP(18) PV_STEP(19)
    lds_arrive(lds, L_CNT + 4, lane);
    PV_STEP(20) PV_STEP(21) PV_STEP(22) PV_STEP(23)
    PV_STEP(24) PV_STEP(25) PV_STEP(26) PV_STEP(27) PV_STEP(28) PV_STEP(29) PV_STEP(30) PV_STEP(31)
#undef PV_STEP
#undef ISSUE
    {
      float u[4], t[2];
#pragma unroll
      for (int i = 0; i < 4; ++i) u[i] = sw16_add(acc[i], acc[i + 4]);
#pragma unroll
      for (int i = 0; i < 2; ++i) t[i] = sw32_add(u[i], u[i + 2]);
      lds_wait(lds, L_CNT + 2, 1);
      const float f = lds[L_F + wave];
      *reinterpret_cast<f32x2*>(lds + L_REC + wave * 128 + 8 * c + 4 * (r & 1) + 2 * (r >> 1)) = f32x2{t[0] * f, t[1] * f};
    }
  } else {
#pragma unroll
    for (int i = 0; i < 4; ++i) __hip_atomic_store((gu64*)mybox + min(lane + 64 * i, BX_CLR / 2 - 1), 0ull, RLX_AGENT);
    asm volatile("" ::: "memory");
    tag = __hip_atomic_load((gu64*)(ws + WS_E), RLX_AGENT);
    u32 xq0, xq1;
    {
      const u16* p = x + CS * s + 8 * (c < 10 ? c : 9) + 2 * r;
      xq0 = *reinterpret_cast<const u32*>(p); xq1 = *reinterpret_cast<const u32*>(p + DM);
    }
    u32 gwr[5], xga[5], xgb[5];
    {
      u32x4 wqr[20];
      const u16* p = wq + (size_t)(CS * s + 2 * r) * (NH * DH) + h * DH + 8 * c;
#pragma unroll
      for (int j = 0; j < 10; ++j) {
        wqr[2 * j] = ldnt(p + (size_t)(8 * j) * (NH * DH));
        wqr[2 * j + 1] = ldnt(p + (size_t)(8 * j + 1) * (NH * DH));
      }
      __builtin_amdgcn_sched_barrier(0);
      __builtin_amdgcn_s_barrier();
      __builtin_amdgcn_sched_barrier(0);
#pragma unroll
      for (int t = 0; t < 5; ++t) {
        gwr[t] = *reinterpret_cast<const u32*>(gw + 2 * lane + 128 * t);
        xga[t] = *reinterpret_cast<const u32*>(x + 2 * lane + 128 * t); xgb[t] = *reinterpret_cast<const u32*>(x + DM + 2 * lane + 128 * t);
      }
      __builtin_amdgcn_sched_barrier(0);
      float qa[16];
#pragma unroll
      for (int j = 0; j < 16; ++j) qa[j] = 0.f;
#define Q_STEP(J) { const u32 x0 = dppu<0x150 + (J)>(xq0), x1 = dppu<0x150 + (J)>(xq1); \
      _Pragma("unroll") for (int k = 0; k < 4; ++k) { \
        const u32 lo = __builtin_amdgcn_perm(wqr[2 * (J) + 1][k], wqr[2 * (J)][k], 0x05040100u); \
        const u32 hi = __builtin_amdgcn_perm(wqr[2 * (J) + 1][k], wqr[2 * (J)][k], 0x07060302u); \
        qa[2 * k] = dot2(lo, x0, qa[2 * k]); qa[2 * k + 1] = dot2(hi, x0, qa[2 * k + 1]); \
        qa[8 + 2 * k] = dot2(lo, x1, qa[8 + 2 * k]); qa[8 + 2 * k + 1] = dot2(hi, x1, qa[8 + 2 * k + 1]); } }
      Q_STEP(0) Q_STEP(1) Q_STEP(2) Q_STEP(3) Q_STEP(4) Q_STEP(5) Q_STEP(6) Q_STEP(7) Q_STEP(8) Q_STEP(9)
#undef Q_STEP
      float u[8], t[4];
#pragma unroll
      for (int i = 0; i < 8; ++i) u[i] = sw16_add(qa[i], qa[i + 8]);
#pragma unroll
      for (int i = 0; i < 4; ++i) t[i] = sw32_add(u[i], u[i + 4]);
      gu32* dst = mybox + BX_Q + (r & 1) * 128 + 8 * c + 4 * (r >> 1);
      put_raw2(dst, t[0], t[1]); put_raw2(dst + 2, t[2], t[3]);
    }
    wait_vm<0>();
    tag = epoch_mix(tag);
    __hip_atomic_store((gu64*)(ws + WS_FLAG + (size_t)bid * 128), tag, RLX_AGENT);
    STAMP(1);
    float g0 = 0.f, g1 = 0.f;
    {
#pragma unroll
      for (int t = 0; t < 5; ++t) {
        g0 += bf16r(bflo(xga[t]) * bflo(gwr[t])); g1 += bf16r(bflo(xgb[t]) * bflo(gwr[t]));
        g0 += bf16r(bfhi(xga[t]) * bfhi(gwr[t])); g1 += bf16r(bfhi(xgb[t]) * bfhi(gwr[t]));
      }
      g0 = x32_sum(x16_sum(row_sum(g0))); g1 = x32_sum(x16_sum(row_sum(g1)));
      g0 = bf16r(g0); g1 = bf16r(g1);
      g0 = bf16r(1.f / (1.f + __expf(-g0))); g1 = bf16r(1.f / (1.f + __expf(-g1)));
      if (lane == 0) { lds[L_G] = g0; lds[L_G + 1] = g1; }
    }
    gu64* fptr = (gu64*)(ws + WS_FLAG + (size_t)(lane < 8 ? lane * 32 + h : s * 32 + ((lane - 8) & 31)) * 128);
    for (u32 spins = 0; spins < SPIN_CAP; ++spins) {
      u64 v = tag;
      if (lane < 8) v = __hip_atomic_load(fptr, RLX_AGENT);
      if (__all(v == tag)) break;
      __builtin_amdgcn_s_sleep(QSLEEP);
    }
    {
      const gu32* qsrc = boxes + (size_t)h * BX_WORDS + BX_Q + (r & 1) * 128 + 8 * c + 4 * (r >> 1);
      constexpr size_t GS = (size_t)32 * BX_WORDS;
      u32x4 g[8];
      asm volatile("global_load_dwordx4 %0, %8, off sc1\n\tglobal_load_dwordx4 %1, %9, off sc1\n\t"
                   "global_load_dwordx4 %2, %10, off sc1\n\tglobal_load_dwordx4 %3, %11, off sc1\n\t"
                   "global_load_dwordx4 %4, %12, off sc1\n\tglobal_load_dwordx4 %5, %13, off sc1\n\t"
                   "global_load_dwordx4 %6, %14, off sc1\n\tglobal_load_dwordx4 %7, %15, off sc1\n\ts_waitcnt vmcnt(0)"
                   : "=&v"(g[0]), "=&v"(g[1]), "=&v"(g[2]), "=&v"(g[3]), "=&v"(g[4]), "=&v"(g[5]), "=&v"(g[6]), "=&v"(g[7])
                   : "v"(qsrc), "v"(qsrc + GS), "v"(qsrc + 2 * GS), "v"(qsrc + 3 * GS), "v"(qsrc + 4 * GS), "v"(qsrc + 5 * GS), "v"(qsrc + 6 * GS), "v"(qsrc + 7 * GS)
                   : "memory");
      STAMP(2);
      f32x4 q = {0.f, 0.f, 0.f, 0.f};
#pragma unroll
      for (int sp = 0; sp < 8; ++sp) q += __builtin_bit_cast(f32x4, g[sp]);
      u32x2 pk = {(u32)bf16bits(q[0]) | ((u32)bf16bits(q[1]) << 16), (u32)bf16bits(q[2]) | ((u32)bf16bits(q[3]) << 16)};
      *reinterpret_cast<u32x2*>(lds + L_QB + (r & 1) * 64 + 4 * c + 2 * (r >> 1)) = pk;
      lds_arrive(lds, L_CNT, lane);
    }
    STAMP(3);
    lds_wait(lds, L_CNT + 3, 8);
    {
      f32x2 u = {0.f, 0.f};
#pragma unroll
      for (int w = 0; w < 8; ++w) u += *reinterpret_cast<const f32x2*>(lds + L_UP + w * 128 + 2 * lane);
      put_raw2(mybox + BX_U + 2 * lane, u[0], u[1]);
      wait_vm<0>();
      __hip_atomic_store((gu64*)(ws + WS_FLAG + (size_t)bid * 128) + 1, tag, RLX_AGENT);
    }
    lds_wait(lds, L_CNT + 1, 8);
    const int wl = lane & 7;
    const f32x2 ml = *reinterpret_cast<const f32x2*>(lds + L_ML + 2 * wl);
    {
      float M = fmaxf(ml[0], dpp<XOR1>(ml[0])); M = fmaxf(M, dpp<XOR2>(M));
      float L = ml[1] * __builtin_amdgcn_exp2f((ml[0] - M) * 1.4426950408889634f);
      L += dpp<XOR1>(L); L += dpp<XOR2>(L);
      if (lane == 0 || lane == 4) put_word2(mybox + BX_ML + (lane >> 1), M, L);
    }
    for (u32 spins = 0; spins < SPIN_CAP; ++spins) {
      u64 v = tag;
      if (lane < 8) v = __hip_atomic_load(fptr + 1, RLX_AGENT);
      if (__all(v == tag)) break;
      __builtin_amdgcn_s_sleep(16);
    }
    {
      u64 ug[8];
#pragma unroll
      for (int sp = 0; sp < 8; ++sp)
        ug[sp] = __hip_atomic_load((gu64*)(boxes + (size_t)(sp * 32 + h) * BX_WORDS + BX_U) + lane, RLX_AGENT);
      float u0 = 0.f, u1 = 0.f;
#pragma unroll
      for (int sp = 0; sp < 8; ++sp) { u0 += __uint_as_float((u32)ug[sp]); u1 += __uint_as_float((u32)(ug[sp] >> 32)); }
      u0 = bf16r(u0); u1 = bf16r(u1);
      u16* ob = reinterpret_cast<u16*>(lds + L_OB);
      ob[DH + lane] = has_f ? bf16bits(u0 / (1.f + __expf(-u0))) : (u16)0;
      ob[192 + DH + lane] = has_f ? bf16bits(u1 / (1.f + __expf(-u1))) : (u16)0;
    }
    for (u32 spins = 0; spins < SPIN_CAP; ++spins) {
      const u64 fl = __hip_atomic_load(fptr, RLX_AGENT);
      if (__all(lane >= 40 || fl == tag)) break;
      __builtin_amdgcn_s_sleep(16);
    }
    {
      lds_wait(lds, L_CNT + 4, 1);
      const gu32* msrc = boxes + (size_t)(wl * 32 + h) * BX_WORDS + BX_ML;
      u32x4 gm;
      for (u32 spins = 0; spins < SPIN_CAP; ++spins) {
        gm = get_word4(msrc);
        if (__all(all4(gm) & 1u)) break;
        __builtin_amdgcn_s_sleep(MSLEEP);
      }
      float fsc[2];
#pragma unroll
      for (int bb = 0; bb < 2; ++bb) {
        const float Ms = __uint_as_float(gm[2 * bb]), Ls = __uint_as_float(gm[2 * bb + 1]);
        float Mg = fmaxf(Ms, dpp<XOR1>(Ms)); Mg = fmaxf(Mg, dpp<XOR2>(Mg)); Mg = fmaxf(Mg, dpp<XOR7>(Mg));
        float Lg = Ls * __builtin_amdgcn_exp2f((Ms - Mg) * 1.4426950408889634f);
        Lg += dpp<XOR1>(Lg); Lg += dpp<XOR2>(Lg); Lg += dpp<XOR7>(Lg);
        fsc[bb] = __builtin_amdgcn_exp2f((ml[0] - Mg) * 1.4426950408889634f) / Lg;
      }
      if (lane < 8) lds[L_F + lane] = lane < 4 ? fsc[0] : fsc[1];
      lds_arrive(lds, L_CNT + 2, lane);
    }
    STAMP(4);
  }
  if (wave < 8) STAMP(4);
  __syncthreads();
  STAMP(5);

  if (wave < 4) {
    const float* rec = lds + L_REC + (tid >> 7) * 512 + (tid & 127);
    put_word(mybox + BX_A + tid, (rec[0] + rec[128]) + (rec[256] + rec[384]));
  }
  u32x4 wg[6];
  {
    float* wsrc = lds + L_WT + (8 * r + (c >> 2)) * WT_STRIDE + 8 * (wave < 5 ? wave : 4) + 2 * (c & 3);
#pragma unroll
    for (int ks = 0; ks < 6; ++ks) {
      const s16x4 lo = __builtin_amdgcn_ds_read_tr16_b64_v4i16((LAS s16x4*)(wsrc + (32 * ks) * WT_STRIDE));
      const s16x4 hi = __builtin_amdgcn_ds_read_tr16_b64_v4i16((LAS s16x4*)(wsrc + (32 * ks + 4) * WT_STRIDE));
      const u32x2 l2 = __builtin_bit_cast(u32x2, lo), h2 = __builtin_bit_cast(u32x2, hi);
      wg[ks] = u32x4{l2[0], l2[1], h2[0], h2[1]};
    }
  }
  if (wave < 8) {
    const gu32* src = boxes + (size_t)((lane >> 3) * 32 + h) * BX_WORDS + BX_A + 32 * wave + 4 * (lane & 7);
    u32x4 ga;
    for (u32 spins = 0; spins < SPIN_CAP; ++spins) {
      ga = get_word4(src);
      if (__all(all4(ga) & 1u)) break;
      if (ASLEEP) __builtin_amdgcn_s_sleep(ASLEEP);
    }
    float o[4];
#pragma unroll
    for (int k = 0; k < 4; ++k) { float t = __uint_as_float(ga[k]); t += dpp<XOR8>(t); o[k] = x32_sum(x16_sum(t)); }
    if (lane < 8) {
      u32x2 pk = {(u32)bf16bits(o[0]) | ((u32)bf16bits(o[1]) << 16), (u32)bf16bits(o[2]) | ((u32)bf16bits(o[3]) << 16)};
      *reinterpret_cast<u32x2*>(lds + L_OB + (wave >> 2) * 96 + 16 * (wave & 3) + 2 * lane) = pk;
    }
  }
  __syncthreads();
  STAMP(6);

  if (wave < 5) {
    f32x4 ya = {0.f, 0.f, 0.f, 0.f}, yb = {0.f, 0.f, 0.f, 0.f};
    const float* arow = lds + L_OB + (c & 1) * 96 + 4 * r;
#pragma unroll
    for (int ks = 0; ks < 6; ++ks) {
      u32x4 af = *reinterpret_cast<const u32x4*>(arow + 16 * ks);
      if (c >= 2) af = u32x4{0u, 0u, 0u, 0u};
      if (ks < 4) ya = __builtin_amdgcn_mfma_f32_16x16x32_bf16(__builtin_bit_cast(bf16x8, af), __builtin_bit_cast(bf16x8, wg[ks]), ya, 0, 0, 0);
      else yb = __builtin_amdgcn_mfma_f32_16x16x32_bf16(__builtin_bit_cast(bf16x8, af), __builtin_bit_cast(bf16x8, wg[ks]), yb, 0, 0, 0);
    }
    if (lane < 16) {
      const float g0 = lds[L_G], g1 = lds[L_G + 1];
      put_word(mybox + BX_B + 16 * wave + lane, fmaf(g0, ya[0], bf16r(1.f - g0) * yb[0]));
      put_word(mybox + BX_B + CS + 16 * wave + lane, fmaf(g1, ya[1], bf16r(1.f - g1) * yb[1]));
    }
  }
  STAMP(7);
  if (tid < 160) {
    const int hp = tid & 31, idx = 5 * h + (tid >> 5);
    gu32* g = boxes + (size_t)(s * 32 + hp) * BX_WORDS + BX_B + idx;
    u32 gv = 0;
    for (u32 spins = 0; spins < SPIN_CAP; ++spins) {
      gv = __hip_atomic_load(g, RLX_AGENT);
      if (__all(gv & 1u)) break;
      __builtin_amdgcn_s_sleep(BSLEEP);
    }
    const float t = x16_sum(row_sum(__uint_as_float(gv)));
    if (hp == 0) { const int bb = idx / CS, col = idx - bb * CS; out[bb * DM + CS * s + col] = bf16bits(t); }
  }
  if (bid == 0) {
    __syncthreads();
    if (tid == 512) __hip_atomic_store((gu64*)(ws + WS_E), tag, RLX_AGENT);
  }
}

extern "C" void kernel_launch(void* const* d_in, const int* in_sizes, int n_in,
                              void* d_out, int out_size, void* d_ws, size_t ws_size,
                              hipStream_t stream) {
  const u16* x = (const u16*)d_in[0];
  const u16* kc = (const u16*)d_in[1];
  const u16* vc = (const u16*)d_in[2];
  const u16* wq = (const u16*)d_in[3];
  const u16* wo = (const u16*)d_in[4];
  const u16* w1 = (const u16*)d_in[6];
  const u16* w2 = (const u16*)d_in[7];
  const u16* gw = (const u16*)d_in[8];
  fused_kernel<<<256, 576, 0, stream>>>(x, kc, vc, wq, wo, w1, w2, gw, (u16*)d_out, (unsigned char*)d_ws);
}
```

```cpp
#include <hip/hip_runtime.h>
#include <hip/hip_bf16.h>
#include <stdint.h>

typedef unsigned int u32;
typedef unsigned short u16;
typedef unsigned long long u64;
using u32x2 = __attribute__((ext_vector_type(2))) unsigned;
using u32x4 = __attribute__((ext_vector_type(4))) unsigned;
using f32x2 = __attribute__((ext_vector_type(2))) float;
using f32x4 = __attribute__((ext_vector_type(4))) float;
using bf16x2 = __attribute__((ext_vector_type(2))) __bf16;
#define GAS __attribute__((address_space(1)))
typedef GAS u64 gu64;
typedef GAS u32 gu32;
#define RLX_AGENT __ATOMIC_RELAXED, __HIP_MEMORY_SCOPE_AGENT

constexpr int DM = 640, NH = 32, SK = 4096, DH = 128, FF = 1728;
constexpr int NS = 8;
constexpr int FH = 64;
constexpr int NFG = FF / FH;
constexpr int CS = DM / NS;
constexpr size_t WS_E = 0;
constexpr size_t WS_FLAG = 4096;
constexpr size_t WS_BOX = 65536;
constexpr int BX_A = 0;
constexpr int BX_ML = 256;
constexpr int BX_B = 288;
constexpr int BX_CLR = 448;
constexpr int BX_Q = 448;
constexpr int BX_U = 704;
constexpr int BX_WORDS = 832;
constexpr u32 SPIN_CAP = 1u << 18;

__device__ __forceinline__ float dot2(u32 k, u32 q, float acc) {
  return __builtin_amdgcn_fdot2_f32_bf16(__builtin_bit_cast(bf16x2, k), __builtin_bit_cast(bf16x2, q), acc, false);
}
__device__ __forceinline__ float bflo(u32 v) { return __builtin_bit_cast(float, v << 16); }
__device__ __forceinline__ float bfhi(u32 v) { return __builtin_bit_cast(float, v & 0xffff0000u); }
__device__ __forceinline__ float bf16r(float x) { return (float)(__bf16)x; }
__device__ __forceinline__ u16 bf16bits(float x) { return __builtin_bit_cast(u16, (__bf16)x); }
template <int CTRL> __device__ __forceinline__ float dpp(float x) {
  return __builtin_bit_cast(float, __builtin_amdgcn_mov_dpp(__builtin_bit_cast(int, x), CTRL, 0xf, 0xf, true));
}
template <int CTRL> __device__ __forceinline__ u32 dppu(u32 x) {
  return (u32)__builtin_amdgcn_mov_dpp((int)x, CTRL, 0xf, 0xf, true);
}
constexpr int XOR1 = 0xB1, XOR2 = 0x4E, XOR7 = 0x141, XOR8 = 0x128;
__device__ __forceinline__ float sw16_add(float a, float b) {
  auto s = __builtin_amdgcn_permlane16_swap(__float_as_uint(a), __float_as_uint(b), false, false);
  return __uint_as_float(s[0]) + __uint_as_float(s[1]);
}
__device__ __forceinline__ float sw32_add(float a, float b) {
  auto t = __builtin_amdgcn_permlane32_swap(__float_as_uint(a), __float_as_uint(b), false, false);
  return __uint_as_float(t[0]) + __uint_as_float(t[1]);
}
__device__ __forceinline__ float x16_sum(float x) { return sw16_add(x, x); }
__device__ __forceinline__ float x32_sum(float x) { return sw32_add(x, x); }
__device__ __forceinline__ float x16_max(float x) {
  auto s = __builtin_amdgcn_permlane16_swap(__float_as_uint(x), __float_as_uint(x), false, false);
  return fmaxf(__uint_as_float(s[0]), __uint_as_float(s[1]));
}
__device__ __forceinline__ float x32_max(float x) {
  auto t = __builtin_amdgcn_permlane32_swap(__float_as_uint(x), __float_as_uint(x), false, false);
  return fmaxf(__uint_as_float(t[0]), __uint_as_float(t[1]));
}
__device__ __forceinline__ float row_sum(float x) {
  x += dpp<XOR1>(x); x += dpp<XOR2>(x); x += dpp<XOR7>(x); x += dpp<XOR8>(x); return x;
}
__device__ __forceinline__ float row_max(float x) {
  x = fmaxf(x, dpp<XOR1>(x)); x = fmaxf(x, dpp<XOR2>(x)); x = fmaxf(x, dpp<XOR7>(x)); x = fmaxf(x, dpp<XOR8>(x)); return x;
}
__device__ __forceinline__ float tr_reduce16(float (&x)[16], int c) {
#define TR_STEP(HALF, CTRL, BIT) _Pragma("unroll") for (int hh = 0; hh < (HALF); ++hh) { \
    const float lo_ = x[hh], hi_ = x[hh + (HALF)]; const float t1_ = lo_ + dpp<CTRL>(lo_), t2_ = hi_ + dpp<CTRL>(hi_); \
    x[hh] = (c & (BIT)) ? t2_ : t1_; }
  TR_STEP(8, XOR8, 8) TR_STEP(4, XOR7, 4) TR_STEP(2, XOR2, 2) TR_STEP(1, XOR1, 1)
#undef TR_STEP
  return x[0];
}
__device__ __forceinline__ void put_word(gu32* g, float v) { __hip_atomic_store(g, __float_as_uint(v) | 1u, RLX_AGENT); }
__device__ __forceinline__ void put_word2(gu32* g, float v0, float v1) {
  __hip_atomic_store((gu64*)g, (u64)(__float_as_uint(v0) | 1u) | ((u64)(__float_as_uint(v1) | 1u) << 32), RLX_AGENT);
}
__device__ __forceinline__ u32x4 get_word4(const gu32* g) {
  u32x4 v;
  asm volatile("global_load_dwordx4 %0, %1, off sc1\n\ts_waitcnt vmcnt(0)" : "=&v"(v) : "v"(g) : "memory");
  return v;
}
__device__ __forceinline__ void put_raw2(gu32* g, float v0, float v1) {
  __hip_atomic_store((gu64*)g, (u64)__float_as_uint(v0) | ((u64)__float_as_uint(v1) << 32), RLX_AGENT);
}
__device__ __forceinline__ u64 epoch_mix(u64 e) { return e * 6364136223846793005ull + 1442695040888963407ull; }

constexpr int L_QB = 0;
constexpr int L_REC = 128;
constexpr int L_OB = L_REC + 8 * 132;
constexpr int L_ML = L_OB + 192;
constexpr int L_F = L_ML + 16;
constexpr int L_G = L_F + 16;
constexpr int L_CNT = L_G + 2;
constexpr int L_UP = L_CNT + 6;
constexpr int L_WT = L_UP + 1024;
constexpr int WT_STRIDE = 44;
constexpr int L_END = L_WT + 192 * WT_STRIDE;
typedef short s16x4 __attribute__((ext_vector_type(4)));
using bf16x8 = __attribute__((ext_vector_type(8))) __bf16;
constexpr int RNG = 24;
constexpr int WIN = 4;
constexpr int AUXAT = 6;
constexpr int DMIN = WIN + 2;
constexpr int BUMP = 2;
constexpr int niss(int i) { const int a = i + 1 + DMIN + ((i >= 31 && i < 31 + BUMP) ? 31 + BUMP - i : 0), b = RNG + (i + 1) / 3; const int m = a > b ? a : b; return m > 64 ? 64 : m; }
#define LAS __attribute__((address_space(3)))
__device__ __forceinline__ u32x4 ldnt(const u16* p) { return __builtin_nontemporal_load(reinterpret_cast<const u32x4*>(p)); }
template <int N> __device__ __forceinline__ void wait_vm() { asm volatile("s_waitcnt vmcnt(%0)" :: "i"(N) : "memory"); }
__device__ __forceinline__ void lds_wait(float* lds, int idx, u32 want) {
  for (u32 spins = 0; spins < (1u << 22); ++spins) {
    if (__builtin_amdgcn_readfirstlane(__hip_atomic_load((LAS u32*)(lds + idx), __ATOMIC_ACQUIRE, __HIP_MEMORY_SCOPE_WORKGROUP)) >= want) break;
    __builtin_amdgcn_s_sleep(1);
  }
}
__device__ __forceinline__ void lds_arrive(float* lds, int idx, int lane) {
  if (lane == 0) __hip_atomic_fetch_add((LAS u32*)(lds + idx), 1u, __ATOMIC_RELEASE, __HIP_MEMORY_SCOPE_WORKGROUP);
}
__device__ __forceinline__ u32 all4(u32x4 v) { return v[0] & v[1] & v[2] & v[3]; }

__global__ void __launch_bounds__(576, 3)
fused_kernel(const u16* __restrict__ x, const u16* __restrict__ kc, const u16* __restrict__ vc,
             const u16* __restrict__ wq, const u16* __restrict__ wo, const u16* __restrict__ w1,
             const u16* __restrict__ w2, const u16* __restrict__ gw, u16* __restrict__ out, unsigned char* ws) {
  __shared__ __attribute__((aligned(16))) float lds[L_END];
  const int tid = threadIdx.x, lane = tid & 63;
  const int wave = __builtin_amdgcn_readfirstlane(tid >> 6);
  const int r = lane >> 4, c = lane & 15;
  const int bk = blockIdx.x >> 5;
  const int s = 4 * (bk & 1) + ((blockIdx.x >> 3) & 3);
  const int h = 8 * (bk >> 1) + ((blockIdx.x + 3 * bk) & 7);
  const bool has_f = h < NFG;
  const int bid = s * 32 + h;
  if (tid < 6) reinterpret_cast<u32*>(lds)[L_CNT + tid] = 0u;
  gu32* boxes = (gu32*)(ws + WS_BOX);
  gu32* mybox = boxes + (size_t)bid * BX_WORDS;
  u64 tag = 0;

  if (wave < 8) {
    const int b = wave >> 2, wv = wave & 3;
    const size_t kvoff = ((size_t)(b * NH + h) * SK + s * (SK / NS) + 4 * wv + r) * DH + 8 * c;
    const u16* kp = kc + kvoff;
    const u16* vp = vc + kvoff;
    u32x4 ring[RNG];
    asm volatile("s_waitcnt lgkmcnt(0)" ::: "memory");
    __builtin_amdgcn_s_barrier();
    __builtin_amdgcn_sched_barrier(0);
    u32x4 w1r[2];
    u32 xm[2][2];
    {
      const int col0 = (has_f ? FH * h : 0) + 8 * (lane & 7);
#pragma unroll
      for (int t = 0; t < 2; ++t) {
        const int rr = (lane >> 3) + 8 * t, row = CS * s + 10 * wave + (rr < 10 ? rr : 9);
        xm[t][0] = x[row]; xm[t][1] = x[DM + row];
      }
      __builtin_amdgcn_sched_barrier(0);
#pragma unroll
      for (int t = 0; t < 2; ++t) {
        const int rr = (lane >> 3) + 8 * t, row = CS * s + 10 * wave + (rr < 10 ? rr : 9);
        w1r[t] = ldnt(w1 + (size_t)row * FF + col0);
      }
    }
    const int stg = tid < 480 ? tid : 479;
    const int srg = stg / 10, sch = stg - 10 * srg;
    u32x4 wst[4];
#pragma unroll
    for (int j = 0; j < 4; ++j) {
      const int row = srg + 48 * j;
      const u16* p = row < DH ? wo + (size_t)(h * DH + row) * DM : (has_f ? w2 + (size_t)(FH * h + row - DH) * DM : wo + (size_t)(h * DH + srg) * DM);
      wst[j] = ldnt(p + CS * s + 8 * sch);
    }
    __builtin_amdgcn_sched_barrier(0);
#define ISSUE(N) { if (!((N) > 32 + DMIN - 1 && (N) < 32 + DMIN + BUMP)) wait_vm<WIN - 1>(); \
      if ((N) < 32) ring[(N) % RNG] = ldnt(kp + (size_t)(16 * (N)) * DH); else ring[(N) % RNG] = ldnt(vp + (size_t)(16 * ((N) - 32)) * DH); \
      __builtin_amdgcn_sched_barrier(0); }
#pragma unroll
    for (int i = 0; i < AUXAT; ++i) ISSUE(i)
    {
#pragma unroll
      for (int j = 0; j < 4; ++j) {
        const int row = srg + 48 * j;
        *reinterpret_cast<u32x4*>(lds + L_WT + row * WT_STRIDE + 4 * sch) = (row < DH || has_f) ? wst[j] : u32x4{0u, 0u, 0u, 0u};
      }
      float u[16];
      const float m1 = lane < 16 ? 1.f : 0.f;
#pragma unroll
      for (int bb = 0; bb < 2; ++bb) {
        const float x0 = bflo(xm[0][bb]), x1 = bflo(xm[1][bb]) * m1;
#pragma unroll
        for (int k = 0; k < 4; ++k) {
          u[bb * 8 + 2 * k] = fmaf(x0, bflo(w1r[0][k]), x1 * bflo(w1r[1][k]));
          u[bb * 8 + 2 * k + 1] = fmaf(x0, bfhi(w1r[0][k]), x1 * bfhi(w1r[1][k]));
        }
      }
      float v8[8], v4[4];
#pragma unroll
      for (int i = 0; i < 16; ++i) u[i] += dpp<XOR8>(u[i]);
#pragma unroll
      for (int i = 0; i < 8; ++i) v8[i] = sw16_add(u[i], u[i + 8]);
#pragma unroll
      for (int i = 0; i < 4; ++i) v4[i] = sw32_add(v8[i], v8[i + 4]);
      if (c < 8) {
        float* dst = lds + L_UP + wave * 128 + 2 * (8 * c + 4 * (r >> 1)) + (r & 1);
#pragma unroll
        for (int i = 0; i < 4; ++i) dst[2 * i] = v4[i];
      }
      lds_arrive(lds, L_CNT + 3, lane);
    }
#pragma unroll
    for (int i = AUXAT; i < RNG; ++i) ISSUE(i)
    lds_wait(lds, L_CNT, 1);
    const u32x4 qp = *reinterpret_cast<const u32x4*>(lds + L_QB + b * 64 + 4 * c);
    float sc[2][16];
#pragma unroll
    for (int i = 0; i < 32; ++i) {
      float d = 0.f;
#pragma unroll
      for (int k = 0; k < 4; ++k) d = dot2(ring[i % RNG][k], qp[k], d);
      asm volatile("" : "+v"(d));
      sc[i >> 4][i & 15] = d;
#pragma unroll
      for (int n = niss(i - 1); n < niss(i); ++n) ISSUE(n)
    }
    float p0, p1, mw, lw;
    {
      float s0 = tr_reduce16(sc[0], c), s1 = tr_reduce16(sc[1], c);
      s0 = bf16r(bf16r(s0) * 0.08838834764831845f);
      s1 = bf16r(bf16r(s1) * 0.08838834764831845f);
      mw = x32_max(x16_max(row_max(fmaxf(s0, s1))));
      p0 = __builtin_amdgcn_exp2f((s0 - mw) * 1.4426950408889634f);
      p1 = __builtin_amdgcn_exp2f((s1 - mw) * 1.4426950408889634f);
      lw = x32_sum(x16_sum(row_sum(p0 + p1)));
      if (lane == 0) *reinterpret_cast<f32x2*>(lds + L_ML + 2 * wave) = f32x2{mw, lw};
      lds_arrive(lds, L_CNT + 1, lane);
    }
    float acc[8];
#pragma unroll
    for (int j = 0; j < 8; ++j) acc[j] = 0.f;
#define PV_STEP(I) { const float pi = dpp<0x150 + ((I) & 15)>((I) < 16 ? p0 : p1); const u32x4 v = ring[(32 + (I)) % RNG]; \
      _Pragma("unroll") for (int k = 0; k < 4; ++k) { acc[2 * k] = fmaf(pi, bflo(v[k]), acc[2 * k]); acc[2 * k + 1] = fmaf(pi, bfhi(v[k]), acc[2 * k + 1]); } \
      asm volatile("" : "+v"(acc[0]), "+v"(acc[1]), "+v"(acc[2]), "+v"(acc[3]), "+v"(acc[4]), "+v"(acc[5]), "+v"(acc[6]), "+v"(acc[7])); \
      _Pragma("unroll") for (int n = niss(31 + (I)); n < niss(32 + (I)); ++n) ISSUE(n) }
    PV_STEP(0) PV_STEP(1) PV_STEP(2) PV_STEP(3) PV_STEP(4) PV_STEP(5) PV_STEP(6) PV_STEP(7)
    PV_STEP(8) PV_STEP(9) PV_STEP(10) PV_STEP(11) PV_STEP(12) PV_STEP(13) PV_STEP(14) PV_STEP(15)
    PV_STEP(16) PV_STEP(17) PV_STEP(18) PV_STEP(19)
    lds_arrive(lds, L_CNT + 4, lane);
    PV_STEP(20) PV_STEP(21) PV_STEP(22) PV_STEP(23)
    PV_STEP(24) PV_STEP(25) PV_STEP(26) PV_STEP(27) PV_STEP(28) PV_STEP(29) PV_STEP(30) PV_STEP(31)
#undef PV_STEP
#undef ISSUE
    {
      float u[4], t[2];
#pragma unroll
      for (int i = 0; i < 4; ++i) u[i] = sw16_add(acc[i], acc[i + 4]);
#pragma unroll
      for (int i = 0; i < 2; ++i) t[i] = sw32_add(u[i], u[i + 2]);
      lds_wait(lds, L_CNT + 2, 1);
      const float f = lds[L_F + wave];
      *reinterpret_cast<f32x2*>(lds + L_REC + wave * 128 + 8 * c + 4 * (r & 1) + 2 * (r >> 1)) = f32x2{t[0] * f, t[1] * f};
    }
  } else {
#pragma unroll
    for (int i = 0; i < 4; ++i) __hip_atomic_store((gu64*)mybox + min(lane + 64 * i, BX_CLR / 2 - 1), 0ull, RLX_AGENT);
    asm volatile("" ::: "memory");
    tag = __hip_atomic_load((gu64*)(ws + WS_E), RLX_AGENT);
    u32 xq0, xq1;
    {
      const u16* p = x + CS * s + 8 * (c < 10 ? c : 9) + 2 * r;
      xq0 = *reinterpret_cast<const u32*>(p); xq1 = *reinterpret_cast<const u32*>(p + DM);
    }
    u32 gwr[5], xga[5], xgb[5];
    {
      u32x4 wqr[20];
      const u16* p = wq + (size_t)(CS * s + 2 * r) * (NH * DH) + h * DH + 8 * c;
#pragma unroll
      for (int j = 0; j < 10; ++j) {
        wqr[2 * j] = ldnt(p + (size_t)(8 * j) * (NH * DH));
        wqr[2 * j + 1] = ldnt(p + (size_t)(8 * j + 1) * (NH * DH));
      }
      __builtin_amdgcn_sched_barrier(0);
      __builtin_amdgcn_s_barrier();
      __builtin_amdgcn_sched_barrier(0);
#pragma unroll
      for (int t = 0; t < 5; ++t) {
        gwr[t] = *reinterpret_cast<const u32*>(gw + 2 * lane + 128 * t);
        xga[t] = *reinterpret_cast<const u32*>(x + 2 * lane + 128 * t); xgb[t] = *reinterpret_cast<const u32*>(x + DM + 2 * lane + 128 * t);
      }
      __builtin_amdgcn_sched_barrier(0);
      float qa[16];
#pragma unroll
      for (int j = 0; j < 16; ++j) qa[j] = 0.f;
#define Q_STEP(J) { const u32 x0 = dppu<0x150 + (J)>(xq0), x1 = dppu<0x150 + (J)>(xq1); \
      _Pragma("unroll") for (int k = 0; k < 4; ++k) { \
        const u32 lo = __builtin_amdgcn_perm(wqr[2 * (J) + 1][k], wqr[2 * (J)][k], 0x05040100u); \
        const u32 hi = __builtin_amdgcn_perm(wqr[2 * (J) + 1][k], wqr[2 * (J)][k], 0x07060302u); \
        qa[2 * k] = dot2(lo, x0, qa[2 * k]); qa[2 * k + 1] = dot2(hi, x0, qa[2 * k + 1]); \
        qa[8 + 2 * k] = dot2(lo, x1, qa[8 + 2 * k]); qa[8 + 2 * k + 1] = dot2(hi, x1, qa[8 + 2 * k + 1]); } }
      Q_STEP(0) Q_STEP(1) Q_STEP(2) Q_STEP(3) Q_STEP(4) Q_STEP(5) Q_STEP(6) Q_STEP(7) Q_STEP(8) Q_STEP(9)
#undef Q_STEP
      float u[8], t[4];
#pragma unroll
      for (int i = 0; i < 8; ++i) u[i] = sw16_add(qa[i], qa[i + 8]);
#pragma unroll
      for (int i = 0; i < 4; ++i) t[i] = sw32_add(u[i], u[i + 4]);
      gu32* dst = mybox + BX_Q + (r & 1) * 128 + 8 * c + 4 * (r >> 1);
      put_raw2(dst, t[0], t[1]); put_raw2(dst + 2, t[2], t[3]);
    }
    wait_vm<0>();
    tag = epoch_mix(tag);
    __hip_atomic_store((gu64*)(ws + WS_FLAG + (size_t)bid * 128), tag, RLX_AGENT);
    float g0 = 0.f, g1 = 0.f;
    {
#pragma unroll
      for (int t = 0; t < 5; ++t) {
        g0 += bf16r(bflo(xga[t]) * bflo(gwr[t])); g1 += bf16r(bflo(xgb[t]) * bflo(gwr[t]));
        g0 += bf16r(bfhi(xga[t]) * bfhi(gwr[t])); g1 += bf16r(bfhi(xgb[t]) * bfhi(gwr[t]));
      }
      g0 = x32_sum(x16_sum(row_sum(g0))); g1 = x32_sum(x16_sum(row_sum(g1)));
      g0 = bf16r(g0); g1 = bf16r(g1);
      g0 = bf16r(1.f / (1.f + __expf(-g0))); g1 = bf16r(1.f / (1.f + __expf(-g1)));
      if (lane == 0) { lds[L_G] = g0; lds[L_G + 1] = g1; }
    }
    gu64* fptr = (gu64*)(ws + WS_FLAG + (size_t)(lane < 8 ? lane * 32 + h : s * 32 + ((lane - 8) & 31)) * 128);
    for (u32 spins = 0; spins < SPIN_CAP; ++spins) {
      u64 v = tag;
      if (lane < 8) v = __hip_atomic_load(fptr, RLX_AGENT);
      if (__all(v == tag)) break;
      __builtin_amdgcn_s_sleep(2);
    }
    asm volatile("" ::: "memory");
    {
      const gu32* qsrc = boxes + (size_t)h * BX_WORDS + BX_Q + (r & 1) * 128 + 8 * c + 4 * (r >> 1);
      constexpr size_t GS = (size_t)32 * BX_WORDS;
      u32x4 g[8];
      asm volatile("global_load_dwordx4 %0, %8, off sc1\n\tglobal_load_dwordx4 %1, %9, off sc1\n\t"
                   "global_load_dwordx4 %2, %10, off sc1\n\tglobal_load_dwordx4 %3, %11, off sc1\n\t"
                   "global_load_dwordx4 %4, %12, off sc1\n\tglobal_load_dwordx4 %5, %13, off sc1\n\t"
                   "global_load_dwordx4 %6, %14, off sc1\n\tglobal_load_dwordx4 %7, %15, off sc1\n\ts_waitcnt vmcnt(0)"
                   : "=&v"(g[0]), "=&v"(g[1]), "=&v"(g[2]), "=&v"(g[3]), "=&v"(g[4]), "=&v"(g[5]), "=&v"(g[6]), "=&v"(g[7])
                   : "v"(qsrc), "v"(qsrc + GS), "v"(qsrc + 2 * GS), "v"(qsrc + 3 * GS), "v"(qsrc + 4 * GS), "v"(qsrc + 5 * GS), "v"(qsrc + 6 * GS), "v"(qsrc + 7 * GS)
                   : "memory");
      f32x4 q = {0.f, 0.f, 0.f, 0.f};
#pragma unroll
      for (int sp = 0; sp < 8; ++sp) q += __builtin_bit_cast(f32x4, g[sp]);
      u32x2 pk = {(u32)bf16bits(q[0]) | ((u32)bf16bits(q[1]) << 16), (u32)bf16bits(q[2]) | ((u32)bf16bits(q[3]) << 16)};
      *reinterpret_cast<u32x2*>(lds + L_QB + (r & 1) * 64 + 4 * c + 2 * (r >> 1)) = pk;
      lds_arrive(lds, L_CNT, lane);
    }
    lds_wait(lds, L_CNT + 3, 8);
    {
      f32x2 u = {0.f, 0.f};
#pragma unroll
      for (int w = 0; w < 8; ++w) u += *reinterpret_cast<const f32x2*>(lds + L_UP + w * 128 + 2 * lane);
      put_raw2(mybox + BX_U + 2 * lane, u[0], u[1]);
      wait_vm<0>();
      __hip_atomic_store((gu64*)(ws + WS_FLAG + (size_t)bid * 128) + 1, tag, RLX_AGENT);
    }
    lds_wait(lds, L_CNT + 1, 8);
    const int wl = lane & 7;
    const f32x2 ml = *reinterpret_cast<const f32x2*>(lds + L_ML + 2 * wl);
    {
      float M = fmaxf(ml[0], dpp<XOR1>(ml[0])); M = fmaxf(M, dpp<XOR2>(M));
      float L = ml[1] * __builtin_amdgcn_exp2f((ml[0] - M) * 1.4426950408889634f);
      L += dpp<XOR1>(L); L += dpp<XOR2>(L);
      if (lane == 0 || lane == 4) put_word2(mybox + BX_ML + (lane >> 1), M, L);
    }
    for (u32 spins = 0; spins < SPIN_CAP; ++spins) {
      u64 v = tag;
      if (lane < 8) v = __hip_atomic_load(fptr + 1, RLX_AGENT);
      if (__all(v == tag)) break;
      __builtin_amdgcn_s_sleep(16);
    }
    asm volatile("" ::: "memory");
    {
      u64 ug[8];
#pragma unroll
      for (int sp = 0; sp < 8; ++sp)
        ug[sp] = __hip_atomic_load((gu64*)(boxes + (size_t)(sp * 32 + h) * BX_WORDS + BX_U) + lane, RLX_AGENT);
      float u0 = 0.f, u1 = 0.f;
#pragma unroll
      for (int sp = 0; sp < 8; ++sp) { u0 += __uint_as_float((u32)ug[sp]); u1 += __uint_as_float((u32)(ug[sp] >> 32)); }
      u0 = bf16r(u0); u1 = bf16r(u1);
      u16* ob = reinterpret_cast<u16*>(lds + L_OB);
      ob[DH + lane] = has_f ? bf16bits(u0 / (1.f + __expf(-u0))) : (u16)0;
      ob[192 + DH + lane] = has_f ? bf16bits(u1 / (1.f + __expf(-u1))) : (u16)0;
    }
    for (u32 spins = 0; spins < SPIN_CAP; ++spins) {
      const u64 fl = __hip_atomic_load(fptr, RLX_AGENT);
      if (__all(lane >= 40 || fl == tag)) break;
      __builtin_amdgcn_s_sleep(16);
    }
    asm volatile("" ::: "memory");
    {
      lds_wait(lds, L_CNT + 4, 1);
      const gu32* msrc = boxes + (size_t)(wl * 32 + h) * BX_WORDS + BX_ML;
      u32x4 gm;
      for (u32 spins = 0; spins < SPIN_CAP; ++spins) {
        gm = get_word4(msrc);
        if (__all(all4(gm) & 1u)) break;
        __builtin_amdgcn_s_sleep(16);
      }
      float fsc[2];
#pragma unroll
      for (int bb = 0; bb < 2; ++bb) {
        const float Ms = __uint_as_float(gm[2 * bb]), Ls = __uint_as_float(gm[2 * bb + 1]);
        float Mg = fmaxf(Ms, dpp<XOR1>(Ms)); Mg = fmaxf(Mg, dpp<XOR2>(Mg)); Mg = fmaxf(Mg, dpp<XOR7>(Mg));
        float Lg = Ls * __builtin_amdgcn_exp2f((Ms - Mg) * 1.4426950408889634f);
        Lg += dpp<XOR1>(Lg); Lg += dpp<XOR2>(Lg); Lg += dpp<XOR7>(Lg);
        fsc[bb] = __builtin_amdgcn_exp2f((ml[0] - Mg) * 1.4426950408889634f) / Lg;
      }
      if (lane < 8) lds[L_F + lane] = lane < 4 ? fsc[0] : fsc[1];
      lds_arrive(lds, L_CNT + 2, lane);
    }
  }
  __syncthreads();

  if (wave < 4) {
    const float* rec = lds + L_REC + (tid >> 7) * 512 + (tid & 127);
    put_word(mybox + BX_A + tid, (rec[0] + rec[128]) + (rec[256] + rec[384]));
  }
  u32x4 wg[6];
  {
    float* wsrc = lds + L_WT + (8 * r + (c >> 2)) * WT_STRIDE + 8 * (wave < 5 ? wave : 4) + 2 * (c & 3);
#pragma unroll
    for (int ks = 0; ks < 6; ++ks) {
      const s16x4 lo = __builtin_amdgcn_ds_read_tr16_b64_v4i16((LAS s16x4*)(wsrc + (32 * ks) * WT_STRIDE));
      const s16x4 hi = __builtin_amdgcn_ds_read_tr16_b64_v4i16((LAS s16x4*)(wsrc + (32 * ks + 4) * WT_STRIDE));
      const u32x2 l2 = __builtin_bit_cast(u32x2, lo), h2 = __builtin_bit_cast(u32x2, hi);
      wg[ks] = u32x4{l2[0], l2[1], h2[0], h2[1]};
    }
  }
  if (wave < 8) {
    const gu32* src = boxes + (size_t)((lane >> 3) * 32 + h) * BX_WORDS + BX_A + 32 * wave + 4 * (lane & 7);
    u32x4 ga;
    for (u32 spins = 0; spins < SPIN_CAP; ++spins) {
      ga = get_word4(src);
      if (__all(all4(ga) & 1u)) break;
    }
    float o[4];
#pragma unroll
    for (int k = 0; k < 4; ++k) { float t = __uint_as_float(ga[k]); t += dpp<XOR8>(t); o[k] = x32_sum(x16_sum(t)); }
    if (lane < 8) {
      u32x2 pk = {(u32)bf16bits(o[0]) | ((u32)bf16bits(o[1]) << 16), (u32)bf16bits(o[2]) | ((u32)bf16bits(o[3]) << 16)};
      *reinterpret_cast<u32x2*>(lds + L_OB + (wave >> 2) * 96 + 16 * (wave & 3) + 2 * lane) = pk;
    }
  }
  __syncthreads();

  if (wave < 5) {
    f32x4 ya = {0.f, 0.f, 0.f, 0.f}, yb = {0.f, 0.f, 0.f, 0.f};
    const float* arow = lds + L_OB + (c & 1) * 96 + 4 * r;
#pragma unroll
    for (int ks = 0; ks < 6; ++ks) {
      u32x4 af = *reinterpret_cast<const u32x4*>(arow + 16 * ks);
      if (c >= 2) af = u32x4{0u, 0u, 0u, 0u};
      if (ks < 4) ya = __builtin_amdgcn_mfma_f32_16x16x32_bf16(__builtin_bit_cast(bf16x8, af), __builtin_bit_cast(bf16x8, wg[ks]), ya, 0, 0, 0);
      else yb = __builtin_amdgcn_mfma_f32_16x16x32_bf16(__builtin_bit_cast(bf16x8, af), __builtin_bit_cast(bf16x8, wg[ks]), yb, 0, 0, 0);
    }
    if (lane < 16) {
      const float g0 = lds[L_G], g1 = lds[L_G + 1];
      put_word(mybox + BX_B + 16 * wave + lane, fmaf(g0, ya[0], bf16r(1.f - g0) * yb[0]));
      put_word(mybox + BX_B + CS + 16 * wave + lane, fmaf(g1, ya[1], bf16r(1.f - g1) * yb[1]));
    }
  }
  if (tid < 160) {
    const int hp = tid & 31, idx = 5 * h + (tid >> 5);
    gu32* g = boxes + (size_t)(s * 32 + hp) * BX_WORDS + BX_B + idx;
    u32 gv = 0;
    for (u32 spins = 0; spins < SPIN_CAP; ++spins) {
      gv = __hip_atomic_load(g, RLX_AGENT);
      if (__all(gv & 1u)) break;
      __builtin_amdgcn_s_sleep(1);
    }
    const float t = x16_sum(row_sum(__uint_as_float(gv)));
    if (hp == 0) { const int bb = idx / CS, col = idx - bb * CS; out[bb * DM + CS * s + col] = bf16bits(t); }
  }
  if (bid == 0) {
    __syncthreads();
    if (tid == 512) __hip_atomic_store((gu64*)(ws + WS_E), tag, RLX_AGENT);
  }
}

extern "C" void kernel_launch(void* const* d_in, const int* in_sizes, int n_in,
                              void* d_out, int out_size, void* d_ws, size_t ws_size,
                              hipStream_t stream) {
  const u16* x = (const u16*)d_in[0];
  const u16* kc = (const u16*)d_in[1];
  const u16* vc = (const u16*)d_in[2];
  const u16* wq = (const u16*)d_in[3];
  const u16* wo = (const u16*)d_in[4];
  const u16* w1 = (const u16*)d_in[6];
  const u16* w2 = (const u16*)d_in[7];
  const u16* gw = (const u16*)d_in[8];
  fused_kernel<<<256, 576, 0, stream>>>(x, kc, vc, wq, wo, w1, w2, gw, (u16*)d_out, (unsigned char*)d_ws);
}
```

```cpp
#include <hip/hip_runtime.h>
#include <hip/hip_bf16.h>
#include <stdint.h>

typedef unsigned int u32;
typedef unsigned short u16;
typedef unsigned long long u64;
using u32x2 = __attribute__((ext_vector_type(2))) unsigned;
using u32x4 = __attribute__((ext_vector_type(4))) unsigned;
using f32x2 = __attribute__((ext_vector_type(2))) float;
using f32x4 = __attribute__((ext_vector_type(4))) float;
using bf16x2 = __attribute__((ext_vector_type(2))) __bf16;
#define GAS __attribute__((address_space(1)))
typedef GAS u64 gu64;
typedef GAS u32 gu32;
#define RLX_AGENT __ATOMIC_RELAXED, __HIP_MEMORY_SCOPE_AGENT

constexpr int DM = 640, NH = 32, SK = 4096, DH = 128, FF = 1728;
constexpr int NS = 8;
constexpr int FH = 64;
constexpr int NFG = FF / FH;
constexpr int CS = DM / NS;
constexpr size_t WS_E = 0;
constexpr size_t WS_FLAG = 4096;
constexpr size_t WS_BOX = 65536;
constexpr int BX_A = 0;
constexpr int BX_ML = 256;
constexpr int BX_B = 288;
constexpr int BX_CLR = 448;
constexpr int BX_Q = 448;
constexpr int BX_U = 704;
constexpr int BX_WORDS = 832;
constexpr u32 SPIN_CAP = 1u << 18;

__device__ __forceinline__ float dot2(u32 k, u32 q, float acc) {
  return __builtin_amdgcn_fdot2_f32_bf16(__builtin_bit_cast(bf16x2, k), __builtin_bit_cast(bf16x2, q), acc, false);
}
__device__ __forceinline__ float bflo(u32 v) { return __builtin_bit_cast(float, v << 16); }
__device__ __forceinline__ float bfhi(u32 v) { return __builtin_bit_cast(float, v & 0xffff0000u); }
__device__ __forceinline__ float bf16r(float x) { return (float)(__bf16)x; }
__device__ __forceinline__ u16 bf16bits(float x) { return __builtin_bit_cast(u16, (__bf16)x); }
template <int CTRL> __device__ __forceinline__ float dpp(float x) {
  return __builtin_bit_cast(float, __builtin_amdgcn_mov_dpp(__builtin_bit_cast(int, x), CTRL, 0xf, 0xf, true));
}
template <int CTRL> __device__ __forceinline__ u32 dppu(u32 x) {
  return (u32)__builtin_amdgcn_mov_dpp((int)x, CTRL, 0xf, 0xf, true);
}
constexpr int XOR1 = 0xB1, XOR2 = 0x4E, XOR7 = 0x141, XOR8 = 0x128;
__device__ __forceinline__ float sw16_add(float a, float b) {
  auto s = __builtin_amdgcn_permlane16_swap(__float_as_uint(a), __float_as_uint(b), false, false);
  return __uint_as_float(s[0]) + __uint_as_float(s[1]);
}
__device__ __forceinline__ float sw32_add(float a, float b) {
  auto t = __builtin_amdgcn_permlane32_swap(__float_as_uint(a), __float_as_uint(b), false, false);
  return __uint_as_float(t[0]) + __uint_as_float(t[1]);
}
__device__ __forceinline__ float x16_sum(float x) { return sw16_add(x, x); }
__device__ __forceinline__ float x32_sum(float x) { return sw32_add(x, x); }
__device__ __forceinline__ float x16_max(float x) {
  auto s = __builtin_amdgcn_permlane16_swap(__float_as_uint(x), __float_as_uint(x), false, false);
  return fmaxf(__uint_as_float(s[0]), __uint_as_float(s[1]));
}
__device__ __forceinline__ float x32_max(float x) {
  auto t = __builtin_amdgcn_permlane32_swap(__float_as_uint(x), __float_as_uint(x), false, false);
  return fmaxf(__uint_as_float(t[0]), __uint_as_float(t[1]));
}
__device__ __forceinline__ float row_sum(float x) {
  x += dpp<XOR1>(x); x += dpp<XOR2>(x); x += dpp<XOR7>(x); x += dpp<XOR8>(x); return x;
}
__device__ __forceinline__ float row_max(float x) {
  x = fmaxf(x, dpp<XOR1>(x)); x = fmaxf(x, dpp<XOR2>(x)); x = fmaxf(x, dpp<XOR7>(x)); x = fmaxf(x, dpp<XOR8>(x)); return x;
}
__device__ __forceinline__ float tr_reduce16(float (&x)[16], int c) {
#define TR_STEP(HALF, CTRL, BIT) _Pragma("unroll") for (int hh = 0; hh < (HALF); ++hh) { \
    const float lo_ = x[hh], hi_ = x[hh + (HALF)]; const float t1_ = lo_ + dpp<CTRL>(lo_), t2_ = hi_ + dpp<CTRL>(hi_); \
    x[hh] = (c & (BIT)) ? t2_ : t1_; }
  TR_STEP(8, XOR8, 8) TR_STEP(4, XOR7, 4) TR_STEP(2, XOR2, 2) TR_STEP(1, XOR1, 1)
#undef TR_STEP
  return x[0];
}
__device__ __forceinline__ void put_word(gu32* g, float v) { __hip_atomic_store(g, __float_as_uint(v) | 1u, RLX_AGENT); }
__device__ __forceinline__ void put_word2(gu32* g, float v0, float v1) {
  __hip_atomic_store((gu64*)g, (u64)(__float_as_uint(v0) | 1u) | ((u64)(__float_as_uint(v1) | 1u) << 32), RLX_AGENT);
}
__device__ __forceinline__ u32x4 get_word4(const gu32* g) {
  u32x4 v;
  asm volatile("global_load_dwordx4 %0, %1, off sc1\n\ts_waitcnt vmcnt(0)" : "=&v"(v) : "v"(g) : "memory");
  return v;
}
__device__ __forceinline__ void put_raw2(gu32* g, float v0, float v1) {
  __hip_atomic_store((gu64*)g, (u64)__float_as_uint(v0) | ((u64)__float_as_uint(v1) << 32), RLX_AGENT);
}
__device__ __forceinline__ void put_raw4(gu32* g, u32x4 v) {
  asm volatile("global_store_dwordx4 %0, %1, off sc1\n\ts_nop 1" :: "v"(g), "v"(v) : "memory");
}
__device__ __forceinline__ u64 epoch_mix(u64 e) { return e * 6364136223846793005ull + 1442695040888963407ull; }

constexpr int L_QB = 0;
constexpr int L_REC = 128;
constexpr int L_OB = L_REC + 8 * 132;
constexpr int L_ML = L_OB + 192;
constexpr int L_F = L_ML + 16;
constexpr int L_G = L_F + 16;
constexpr int L_CNT = L_G + 2;
constexpr int L_UP = L_CNT + 6;
constexpr int L_WT = L_UP + 1024;
constexpr int WT_STRIDE = 44;
constexpr int L_END = L_WT + 192 * WT_STRIDE;
typedef short s16x4 __attribute__((ext_vector_type(4)));
using bf16x8 = __attribute__((ext_vector_type(8))) __bf16;
constexpr int RNG = 24;
constexpr int WIN = 4;
constexpr int AUXAT = 6;
constexpr int DMIN = WIN + 2;
constexpr int BUMP = 2;
constexpr int niss(int i) { const int a = i + 1 + DMIN + ((i >= 31 && i < 31 + BUMP) ? 31 + BUMP - i : 0), b = RNG + (i + 1) / 3; const int m = a > b ? a : b; return m > 64 ? 64 : m; }
#define LAS __attribute__((address_space(3)))
__device__ __forceinline__ u32x4 ldnt(const u16* p) { return __builtin_nontemporal_load(reinterpret_cast<const u32x4*>(p)); }
template <int N> __device__ __forceinline__ void wait_vm() { asm volatile("s_waitcnt vmcnt(%0)" :: "i"(N) : "memory"); }
__device__ __forceinline__ void lds_wait(float* lds, int idx, u32 want) {
  for (u32 spins = 0; spins < (1u << 22); ++spins) {
    if (__builtin_amdgcn_readfirstlane(__hip_atomic_load((LAS u32*)(lds + idx), __ATOMIC_ACQUIRE, __HIP_MEMORY_SCOPE_WORKGROUP)) >= want) break;
    __builtin_amdgcn_s_sleep(1);
  }
}
__device__ __forceinline__ void lds_arrive(float* lds, int idx, int lane) {
  if (lane == 0) __hip_atomic_fetch_add((LAS u32*)(lds + idx), 1u, __ATOMIC_RELEASE, __HIP_MEMORY_SCOPE_WORKGROUP);
}
__device__ __forceinline__ u32 all4(u32x4 v) { return v[0] & v[1] & v[2] & v[3]; }

__global__ void __launch_bounds__(576, 3)
fused_kernel(const u16* __restrict__ x, const u16* __restrict__ kc, const u16* __restrict__ vc,
             const u16* __restrict__ wq, const u16* __restrict__ wo, const u16* __restrict__ w1,
             const u16* __restrict__ w2, const u16* __restrict__ gw, u16* __restrict__ out, unsigned char* ws) {
  __shared__ __attribute__((aligned(16))) float lds[L_END];
  const int tid = threadIdx.x, lane = tid & 63;
  const int wave = __builtin_amdgcn_readfirstlane(tid >> 6);
  const int r = lane >> 4, c = lane & 15;
  const int bk = blockIdx.x >> 5;
  const int s = 4 * (bk & 1) + ((blockIdx.x >> 3) & 3);
  const int h = 8 * (bk >> 1) + ((blockIdx.x + 3 * bk) & 7);
  const bool has_f = h < NFG;
  const int bid = s * 32 + h;
  if (tid < 6) reinterpret_cast<u32*>(lds)[L_CNT + tid] = 0u;
  gu32* boxes = (gu32*)(ws + WS_BOX);
  gu32* mybox = boxes + (size_t)bid * BX_WORDS;
  u64 tag = 0;

  if (wave < 8) {
    const int b = wave >> 2, wv = wave & 3;
    const size_t kvoff = ((size_t)(b * NH + h) * SK + s * (SK / NS) + 4 * wv + r) * DH + 8 * c;
    const u16* kp = kc + kvoff;
    const u16* vp = vc + kvoff;
    u32x4 ring[RNG];
    asm volatile("s_waitcnt lgkmcnt(0)" ::: "memory");
    __builtin_amdgcn_s_barrier();
    __builtin_amdgcn_sched_barrier(0);
    u32x4 w1r[2];
    u32 xm[2][2];
    {
      const int col0 = (has_f ? FH * h : 0) + 8 * (lane & 7);
#pragma unroll
      for (int t = 0; t < 2; ++t) {
        const int rr = (lane >> 3) + 8 * t, row = CS * s + 10 * wave + (rr < 10 ? rr : 9);
        xm[t][0] = x[row]; xm[t][1] = x[DM + row];
      }
      __builtin_amdgcn_sched_barrier(0);
#pragma unroll
      for (int t = 0; t < 2; ++t) {
        const int rr = (lane >> 3) + 8 * t, row = CS * s + 10 * wave + (rr < 10 ? rr : 9);
        w1r[t] = ldnt(w1 + (size_t)row * FF + col0);
      }
    }
    const int stg = tid < 480 ? tid : 479;
    const int srg = stg / 10, sch = stg - 10 * srg;
    u32x4 wst[4];
#pragma unroll
    for (int j = 0; j < 4; ++j) {
      const int row = srg + 48 * j;
      const u16* p = row < DH ? wo + (size_t)(h * DH + row) * DM : (has_f ? w2 + (size_t)(FH * h + row - DH) * DM : wo + (size_t)(h * DH + srg) * DM);
      wst[j] = ldnt(p + CS * s + 8 * sch);
    }
    __builtin_amdgcn_sched_barrier(0);
#define ISSUE(N) { if (!((N) > 32 + DMIN - 1 && (N) < 32 + DMIN + BUMP)) wait_vm<WIN - 1>(); \
      if ((N) < 32) ring[(N) % RNG] = ldnt(kp + (size_t)(16 * (N)) * DH); else ring[(N) % RNG] = ldnt(vp + (size_t)(16 * ((N) - 32)) * DH); \
      __builtin_amdgcn_sched_barrier(0); }
#pragma unroll
    for (int i = 0; i < AUXAT; ++i) ISSUE(i)
    {
#pragma unroll
      for (int j = 0; j < 4; ++j) {
        const int row = srg + 48 * j;
        *reinterpret_cast<u32x4*>(lds + L_WT + row * WT_STRIDE + 4 * sch) = (row < DH || has_f) ? wst[j] : u32x4{0u, 0u, 0u, 0u};
      }
      float u[16];
      const float m1 = lane < 16 ? 1.f : 0.f;
#pragma unroll
      for (int bb = 0; bb < 2; ++bb) {
        const float x0 = bflo(xm[0][bb]), x1 = bflo(xm[1][bb]) * m1;
#pragma unroll
        for (int k = 0; k < 4; ++k) {
          u[bb * 8 + 2 * k] = fmaf(x0, bflo(w1r[0][k]), x1 * bflo(w1r[1][k]));
          u[bb * 8 + 2 * k + 1] = fmaf(x0, bfhi(w1r[0][k]), x1 * bfhi(w1r[1][k]));
        }
      }
      float v8[8], v4[4];
#pragma unroll
      for (int i = 0; i < 16; ++i) u[i] += dpp<XOR8>(u[i]);
#pragma unroll
      for (int i = 0; i < 8; ++i) v8[i] = sw16_add(u[i], u[i + 8]);
#pragma unroll
      for (int i = 0; i < 4; ++i) v4[i] = sw32_add(v8[i], v8[i + 4]);
      if (c < 8) {
        float* dst = lds + L_UP + wave * 128 + 2 * (8 * c + 4 * (r >> 1)) + (r & 1);
#pragma unroll
        for (int i = 0; i < 4; ++i) dst[2 * i] = v4[i];
      }
      lds_arrive(lds, L_CNT + 3, lane);
    }
#pragma unroll
    for (int i = AUXAT; i < RNG; ++i) ISSUE(i)
    lds_wait(lds, L_CNT, 1);
    const u32x4 qp = *reinterpret_cast<const u32x4*>(lds + L_QB + b * 64 + 4 * c);
    float sc[2][16];
#pragma unroll
    for (int i = 0; i < 32; ++i) {
      float d = 0.f;
#pragma unroll
      for (int k = 0; k < 4; ++k) d = dot2(ring[i % RNG][k], qp[k], d);
      asm volatile("" : "+v"(d));
      sc[i >> 4][i & 15] = d;
#pragma unroll
      for (int n = niss(i - 1); n < niss(i); ++n) ISSUE(n)
    }
    float p0, p1, mw, lw;
    {
      float s0 = tr_reduce16(sc[0], c), s1 = tr_reduce16(sc[1], c);
      s0 = bf16r(bf16r(s0) * 0.08838834764831845f);
      s1 = bf16r(bf16r(s1) * 0.08838834764831845f);
      mw = x32_max(x16_max(row_max(fmaxf(s0, s1))));
      p0 = __builtin_amdgcn_exp2f((s0 - mw) * 1.4426950408889634f);
      p1 = __builtin_amdgcn_exp2f((s1 - mw) * 1.4426950408889634f);
      lw = x32_sum(x16_sum(row_sum(p0 + p1)));
      if (lane == 0) *reinterpret_cast<f32x2*>(lds + L_ML + 2 * wave) = f32x2{mw, lw};
      lds_arrive(lds, L_CNT + 1, lane);
    }
    float acc[8];
#pragma unroll
    for (int j = 0; j < 8; ++j) acc[j] = 0.f;
#define PV_STEP(I) { const float pi = dpp<0x150 + ((I) & 15)>((I) < 16 ? p0 : p1); const u32x4 v = ring[(32 + (I)) % RNG]; \
      _Pragma("unroll") for (int k = 0; k < 4; ++k) { acc[2 * k] = fmaf(pi, bflo(v[k]), acc[2 * k]); acc[2 * k + 1] = fmaf(pi, bfhi(v[k]), acc[2 * k + 1]); } \
      asm volatile("" : "+v"(acc[0]), "+v"(acc[1]), "+v"(acc[2]), "+v"(acc[3]), "+v"(acc[4]), "+v"(acc[5]), "+v"(acc[6]), "+v"(acc[7])); \
      _Pragma("unroll") for (int n = niss(31 + (I)); n < niss(32 + (I)); ++n) ISSUE(n) }
    PV_STEP(0) PV_STEP(1) PV_STEP(2) PV_STEP(3) PV_STEP(4) PV_STEP(5) PV_STEP(6) PV_STEP(7)
    PV_STEP(8) PV_STEP(9) PV_STEP(10) PV_STEP(11) PV_STEP(12) PV_STEP(13) PV_STEP(14) PV_STEP(15)
    PV_STEP(16) PV_STEP(17) PV_STEP(18) PV_STEP(19)
    lds_arrive(lds, L_CNT + 4, lane);
    PV_STEP(20) PV_STEP(21) PV_STEP(22) PV_STEP(23)
    PV_STEP(24) PV_STEP(25) PV_STEP(26) PV_STEP(27) PV_STEP(28) PV_STEP(29) PV_STEP(30) PV_STEP(31)
#undef PV_STEP
#undef ISSUE
    {
      float u[4], t[2];
#pragma unroll
      for (int i = 0; i < 4; ++i) u[i] = sw16_add(acc[i], acc[i + 4]);
#pragma unroll
      for (int i = 0; i < 2; ++i) t[i] = sw32_add(u[i], u[i + 2]);
      lds_wait(lds, L_CNT + 2, 1);
      const float f = lds[L_F + wave];
      *reinterpret_cast<f32x2*>(lds + L_REC + wave * 128 + 8 * c + 4 * (r & 1) + 2 * (r >> 1)) = f32x2{t[0] * f, t[1] * f};
    }
  } else {
    put_raw4(mybox + 4 * lane, u32x4{0u, 0u, 0u, 0u});
    put_raw4(mybox + 4 * min(64 + lane, BX_CLR / 4 - 1), u32x4{0u, 0u, 0u, 0u});
    tag = __hip_atomic_load((gu64*)(ws + WS_E), RLX_AGENT);
    u32 xq0, xq1;
    {
      const u16* p = x + CS * s + 8 * (c < 10 ? c : 9) + 2 * r;
      xq0 = *reinterpret_cast<const u32*>(p); xq1 = *reinterpret_cast<const u32*>(p + DM);
    }
    u32 gwr[5], xga[5], xgb[5];
    {
      u32x4 wqr[20];
      const u16* p = wq + (size_t)(CS * s + 2 * r) * (NH * DH) + h * DH + 8 * c;
#pragma unroll
      for (int j = 0; j < 10; ++j) {
        wqr[2 * j] = ldnt(p + (size_t)(8 * j) * (NH * DH));
        wqr[2 * j + 1] = ldnt(p + (size_t)(8 * j + 1) * (NH * DH));
      }
      __builtin_amdgcn_sched_barrier(0);
      __builtin_amdgcn_s_barrier();
      __builtin_amdgcn_sched_barrier(0);
#pragma unroll
      for (int t = 0; t < 5; ++t) {
        gwr[t] = *reinterpret_cast<const u32*>(gw + 2 * lane + 128 * t);
        xga[t] = *reinterpret_cast<const u32*>(x + 2 * lane + 128 * t); xgb[t] = *reinterpret_cast<const u32*>(x + DM + 2 * lane + 128 * t);
      }
      __builtin_amdgcn_sched_barrier(0);
      float qa[16];
#pragma unroll
      for (int j = 0; j < 16; ++j) qa[j] = 0.f;
#define Q_STEP(J) { const u32 x0 = dppu<0x150 + (J)>(xq0), x1 = dppu<0x150 + (J)>(xq1); \
      _Pragma("unroll") for (int k = 0; k < 4; ++k) { \
        const u32 lo = __builtin_amdgcn_perm(wqr[2 * (J) + 1][k], wqr[2 * (J)][k], 0x05040100u); \
        const u32 hi = __builtin_amdgcn_perm(wqr[2 * (J) + 1][k], wqr[2 * (J)][k], 0x07060302u); \
        qa[2 * k] = dot2(lo, x0, qa[2 * k]); qa[2 * k + 1] = dot2(hi, x0, qa[2 * k + 1]); \
        qa[8 + 2 * k] = dot2(lo, x1, qa[8 + 2 * k]); qa[8 + 2 * k + 1] = dot2(hi, x1, qa[8 + 2 * k + 1]); } }
      Q_STEP(0) Q_STEP(1) Q_STEP(2) Q_STEP(3) Q_STEP(4) Q_STEP(5) Q_STEP(6) Q_STEP(7) Q_STEP(8) Q_STEP(9)
#undef Q_STEP
      float u[8], t[4];
#pragma unroll
      for (int i = 0; i < 8; ++i) u[i] = sw16_add(qa[i], qa[i + 8]);
#pragma unroll
      for (int i = 0; i < 4; ++i) t[i] = sw32_add(u[i], u[i + 4]);
      gu32* dst = mybox + BX_Q + (r & 1) * 128 + 8 * c + 4 * (r >> 1);
      put_raw4(dst, u32x4{__float_as_uint(t[0]), __float_as_uint(t[1]), __float_as_uint(t[2]), __float_as_uint(t[3])});
    }
    wait_vm<0>();
    tag = epoch_mix(tag);
    __hip_atomic_store((gu64*)(ws + WS_FLAG + (size_t)bid * 128), tag, RLX_AGENT);
    float g0 = 0.f, g1 = 0.f;
    {
#pragma unroll
      for (int t = 0; t < 5; ++t) {
        g0 += bf16r(bflo(xga[t]) * bflo(gwr[t])); g1 += bf16r(bflo(xgb[t]) * bflo(gwr[t]));
        g0 += bf16r(bfhi(xga[t]) * bfhi(gwr[t])); g1 += bf16r(bfhi(xgb[t]) * bfhi(gwr[t]));
      }
      g0 = x32_sum(x16_sum(row_sum(g0))); g1 = x32_sum(x16_sum(row_sum(g1)));
      g0 = bf16r(g0); g1 = bf16r(g1);
      g0 = bf16r(1.f / (1.f + __expf(-g0))); g1 = bf16r(1.f / (1.f + __expf(-g1)));
      if (lane == 0) { lds[L_G] = g0; lds[L_G + 1] = g1; }
    }
    gu64* fptr = (gu64*)(ws + WS_FLAG + (size_t)(lane < 8 ? lane * 32 + h : s * 32 + ((lane - 8) & 31)) * 128);
    for (u32 spins = 0; spins < SPIN_CAP; ++spins) {
      u64 v = tag;
      if (lane < 8) v = __hip_atomic_load(fptr, RLX_AGENT);
      if (__all(v == tag)) break;
      __builtin_amdgcn_s_sleep(2);
    }
    asm volatile("" ::: "memory");
    {
      const gu32* qsrc = boxes + (size_t)h * BX_WORDS + BX_Q + (r & 1) * 128 + 8 * c + 4 * (r >> 1);
      constexpr size_t GS = (size_t)32 * BX_WORDS;
      u32x4 g[8];
      asm volatile("global_load_dwordx4 %0, %8, off sc1\n\tglobal_load_dwordx4 %1, %9, off sc1\n\t"
                   "global_load_dwordx4 %2, %10, off sc1\n\tglobal_load_dwordx4 %3, %11, off sc1\n\t"
                   "global_load_dwordx4 %4, %12, off sc1\n\tglobal_load_dwordx4 %5, %13, off sc1\n\t"
                   "global_load_dwordx4 %6, %14, off sc1\n\tglobal_load_dwordx4 %7, %15, off sc1\n\ts_waitcnt vmcnt(0)"
                   : "=&v"(g[0]), "=&v"(g[1]), "=&v"(g[2]), "=&v"(g[3]), "=&v"(g[4]), "=&v"(g[5]), "=&v"(g[6]), "=&v"(g[7])
                   : "v"(qsrc), "v"(qsrc + GS), "v"(qsrc + 2 * GS), "v"(qsrc + 3 * GS), "v"(qsrc + 4 * GS), "v"(qsrc + 5 * GS), "v"(qsrc + 6 * GS), "v"(qsrc + 7 * GS)
                   : "memory");
      f32x4 q = {0.f, 0.f, 0.f, 0.f};
#pragma unroll
      for (int sp = 0; sp < 8; ++sp) q += __builtin_bit_cast(f32x4, g[sp]);
      u32x2 pk = {(u32)bf16bits(q[0]) | ((u32)bf16bits(q[1]) << 16), (u32)bf16bits(q[2]) | ((u32)bf16bits(q[3]) << 16)};
      *reinterpret_cast<u32x2*>(lds + L_QB + (r & 1) * 64 + 4 * c + 2 * (r >> 1)) = pk;
      lds_arrive(lds, L_CNT, lane);
    }
    lds_wait(lds, L_CNT + 3, 8);
    {
      f32x2 u = {0.f, 0.f};
#pragma unroll
      for (int w = 0; w < 8; ++w) u += *reinterpret_cast<const f32x2*>(lds + L_UP + w * 128 + 2 * lane);
      put_raw2(mybox + BX_U + 2 * lane, u[0], u[1]);
      wait_vm<0>();
      __hip_atomic_store((gu64*)(ws + WS_FLAG + (size_t)bid * 128) + 1, tag, RLX_AGENT);
    }
    lds_wait(lds, L_CNT + 1, 8);
    const int wl = lane & 7;
    const f32x2 ml = *reinterpret_cast<const f32x2*>(lds + L_ML + 2 * wl);
    {
      float M = fmaxf(ml[0], dpp<XOR1>(ml[0])); M = fmaxf(M, dpp<XOR2>(M));
      float L = ml[1] * __builtin_amdgcn_exp2f((ml[0] - M) * 1.4426950408889634f);
      L += dpp<XOR1>(L); L += dpp<XOR2>(L);
      if (lane == 0 || lane == 4) put_word2(mybox + BX_ML + (lane >> 1), M, L);
    }
    for (u32 spins = 0; spins < SPIN_CAP; ++spins) {
      u64 v = tag;
      if (lane < 8) v = __hip_atomic_load(fptr + 1, RLX_AGENT);
      if (__all(v == tag)) break;
      __builtin_amdgcn_s_sleep(16);
    }
    asm volatile("" ::: "memory");
    {
      u64 ug[8];
#pragma unroll
      for (int sp = 0; sp < 8; ++sp)
        ug[sp] = __hip_atomic_load((gu64*)(boxes + (size_t)(sp * 32 + h) * BX_WORDS + BX_U) + lane, RLX_AGENT);
      float u0 = 0.f, u1 = 0.f;
#pragma unroll
      for (int sp = 0; sp < 8; ++sp) { u0 += __uint_as_float((u32)ug[sp]); u1 += __uint_as_float((u32)(ug[sp] >> 32)); }
      u0 = bf16r(u0); u1 = bf16r(u1);
      u16* ob = reinterpret_cast<u16*>(lds + L_OB);
      ob[DH + lane] = has_f ? bf16bits(u0 / (1.f + __expf(-u0))) : (u16)0;
      ob[192 + DH + lane] = has_f ? bf16bits(u1 / (1.f + __expf(-u1))) : (u16)0;
    }
    for (u32 spins = 0; spins < SPIN_CAP; ++spins) {
      const u64 fl = __hip_atomic_load(fptr, RLX_AGENT);
      if (__all(lane >= 40 || fl == tag)) break;
      __builtin_amdgcn_s_sleep(16);
    }
    asm volatile("" ::: "memory");
    {
      lds_wait(lds, L_CNT + 4, 1);
      const gu32* msrc = boxes + (size_t)(wl * 32 + h) * BX_WORDS + BX_ML;
      u32x4 gm;
      for (u32 spins = 0; spins < SPIN_CAP; ++spins) {
        gm = get_word4(msrc);
        if (__all(all4(gm) & 1u)) break;
        __builtin_amdgcn_s_sleep(16);
      }
      float fsc[2];
#pragma unroll
      for (int bb = 0; bb < 2; ++bb) {
        const float Ms = __uint_as_float(gm[2 * bb]), Ls = __uint_as_float(gm[2 * bb + 1]);
        float Mg = fmaxf(Ms, dpp<XOR1>(Ms)); Mg = fmaxf(Mg, dpp<XOR2>(Mg)); Mg = fmaxf(Mg, dpp<XOR7>(Mg));
        float Lg = Ls * __builtin_amdgcn_exp2f((Ms - Mg) * 1.4426950408889634f);
        Lg += dpp<XOR1>(Lg); Lg += dpp<XOR2>(Lg); Lg += dpp<XOR7>(Lg);
        fsc[bb] = __builtin_amdgcn_exp2f((ml[0] - Mg) * 1.4426950408889634f) / Lg;
      }
      if (lane < 8) lds[L_F + lane] = lane < 4 ? fsc[0] : fsc[1];
      lds_arrive(lds, L_CNT + 2, lane);
    }
  }
  __syncthreads();

  if (wave < 4) {
    const float* rec = lds + L_REC + (tid >> 7) * 512 + (tid & 127);
    put_word(mybox + BX_A + tid, (rec[0] + rec[128]) + (rec[256] + rec[384]));
  }
  u32x4 wg[6];
  {
    float* wsrc = lds + L_WT + (8 * r + (c >> 2)) * WT_STRIDE + 8 * (wave < 5 ? wave : 4) + 2 * (c & 3);
#pragma unroll
    for (int ks = 0; ks < 6; ++ks) {
      const s16x4 lo = __builtin_amdgcn_ds_read_tr16_b64_v4i16((LAS s16x4*)(wsrc + (32 * ks) * WT_STRIDE));
      const s16x4 hi = __builtin_amdgcn_ds_read_tr16_b64_v4i16((LAS s16x4*)(wsrc + (32 * ks + 4) * WT_STRIDE));
      const u32x2 l2 = __builtin_bit_cast(u32x2, lo), h2 = __builtin_bit_cast(u32x2, hi);
      wg[ks] = u32x4{l2[0], l2[1], h2[0], h2[1]};
    }
  }
  if (wave < 8) {
    const gu32* src = boxes + (size_t)((lane >> 3) * 32 + h) * BX_WORDS + BX_A + 32 * wave + 4 * (lane & 7);
    u32x4 ga;
    for (u32 spins = 0; spins < SPIN_CAP; ++spins) {
      ga = get_word4(src);
      if (__all(all4(ga) & 1u)) break;
    }
    float o[4];
#pragma unroll
    for (int k = 0; k < 4; ++k) { float t = __uint_as_float(ga[k]); t += dpp<XOR8>(t); o[k] = x32_sum(x16_sum(t)); }
    if (lane < 8) {
      u32x2 pk = {(u32)bf16bits(o[0]) | ((u32)bf16bits(o[1]) << 16), (u32)bf16bits(o[2]) | ((u32)bf16bits(o[3]) << 16)};
      *reinterpret_cast<u32x2*>(lds + L_OB + (wave >> 2) * 96 + 16 * (wave & 3) + 2 * lane) = pk;
    }
  }
  __syncthreads();

  if (wave < 5) {
    f32x4 ya = {0.f, 0.f, 0.f, 0.f}, yb = {0.f, 0.f, 0.f, 0.f};
    const float* arow = lds + L_OB + (c & 1) * 96 + 4 * r;
#pragma unroll
    for (int ks = 0; ks < 6; ++ks) {
      u32x4 af = *reinterpret_cast<const u32x4*>(arow + 16 * ks);
      if (c >= 2) af = u32x4{0u, 0u, 0u, 0u};
      if (ks < 4) ya = __builtin_amdgcn_mfma_f32_16x16x32_bf16(__builtin_bit_cast(bf16x8, af), __builtin_bit_cast(bf16x8, wg[ks]), ya, 0, 0, 0);
      else yb = __builtin_amdgcn_mfma_f32_16x16x32_bf16(__builtin_bit_cast(bf16x8, af), __builtin_bit_cast(bf16x8, wg[ks]), yb, 0, 0, 0);
    }
    if (lane < 16) {
      const float g0 = lds[L_G], g1 = lds[L_G + 1];
      put_word(mybox + BX_B + 16 * wave + lane, fmaf(g0, ya[0], bf16r(1.f - g0) * yb[0]));
      put_word(mybox + BX_B + CS + 16 * wave + lane, fmaf(g1, ya[1], bf16r(1.f - g1) * yb[1]));
    }
  }
  if (tid < 160) {
    const int hp = tid & 31, idx = 5 * h + (tid >> 5);
    gu32* g = boxes + (size_t)(s * 32 + hp) * BX_WORDS + BX_B + idx;
    u32 gv = 0;
    for (u32 spins = 0; spins < SPIN_CAP; ++spins) {
      gv = __hip_atomic_load(g, RLX_AGENT);
      if (__all(gv & 1u)) break;
      __builtin_amdgcn_s_sleep(1);
    }
    const float t = x16_sum(row_sum(__uint_as_float(gv)));
    if (hp == 0) { const int bb = idx / CS, col = idx - bb * CS; out[bb * DM + CS * s + col] = bf16bits(t); }
  }
  if (bid == 0) {
    __syncthreads();
    if (tid == 512) __hip_atomic_store((gu64*)(ws + WS_E), tag, RLX_AGENT);
  }
}

extern "C" void kernel_launch(void* const* d_in, const int* in_sizes, int n_in,
                              void* d_out, int out_size, void* d_ws, size_t ws_size,
                              hipStream_t stream) {
  const u16* x = (const u16*)d_in[0];
  const u16* kc = (const u16*)d_in[1];
  const u16* vc = (const u16*)d_in[2];
  const u16* wq = (const u16*)d_in[3];
  const u16* wo = (const u16*)d_in[4];
  const u16* w1 = (const u16*)d_in[6];
  const u16* w2 = (const u16*)d_in[7];
  const u16* gw = (const u16*)d_in[8];
  fused_kernel<<<256, 576, 0, stream>>>(x, kc, vc, wq, wo, w1, w2, gw, (u16*)d_out, (unsigned char*)d_ws);
}
```
